```python
import torch

def _softmax(x, axis=-1):
    return torch.softmax(x, dim=axis)

def _gelu(x):
    return torch.nn.functional.gelu(x, approximate='tanh')

def _silu(x):
    return torch.nn.functional.silu(x)

def _sigmoid(x):
    return torch.sigmoid(x)

def _layernorm(x, axis=-1, eps=1e-05):
    m = x.mean(dim=axis, keepdim=True)
    v = x.var(dim=axis, keepdim=True, unbiased=False)
    return (x - m) / torch.sqrt(v + eps)

def _scatter_add(tgt, idx, src, axis=0):
    return tgt.clone().index_add_(axis, idx, src)

def _segment_sum(src, seg, n):
    out = torch.zeros((n,) + src.shape[1:], dtype=src.dtype)
    return out.index_add_(0, seg, src)

def _pad(x, pw):
    flat = tuple((v for pair in reversed(pw) for v in pair))
    return torch.nn.functional.pad(x, flat)
kernel_name = 'sequential_preLN__memory_aug_recurrent_mem_tr__dense_activation_squared_relu__prompt'

def setup_inputs():

    _w = lambda a: (lambda t: t.to(torch.bfloat16) if t.is_floating_point() else t)(torch.as_tensor(a))

    def _A_setup():
        B, S, Dm = (1, 2048, 448)
        H, Dh = (16, 256)
        M, seg = (8, 32)
        seg = min(seg, S)
        S = S // seg * seg
        return {'x': _w((lambda *s: torch.randn(*s, dtype=torch.bfloat16) * 0.05)(B, S, Dm)), 'mem0': _w((lambda *s: torch.randn(*s, dtype=torch.bfloat16) * 0.2)(B, M, Dm)), 'Wq': _w((lambda *s: torch.randn(*s, dtype=torch.bfloat16) * 1.6)(Dm, H, Dh) * Dm ** (-0.5)), 'Wk': _w((lambda *s: torch.randn(*s, dtype=torch.bfloat16) * 1.6)(Dm, H, Dh) * Dm ** (-0.5)), 'Wv': _w((lambda *s: torch.randn(*s, dtype=torch.bfloat16) * 0.2)(Dm, H, Dh) * Dm ** (-0.5)), 'Wo': _w((lambda *s: torch.randn(*s, dtype=torch.bfloat16) * 0.2)(H, Dh, Dm) * (H * Dh) ** (-0.5)), 'seg_len': seg}
    _A_in = _A_setup()

    def _B_setup():
        B, S, D, F = (1, 2048, 448, 1792)
        return {'x': _w((lambda *s: torch.randn(*s, dtype=torch.bfloat16) * 0.05)(B, S, D)), 'w1': _w((lambda *s: torch.randn(*s, dtype=torch.bfloat16) * 0.05)(D, F)), 'b1': _w((lambda *s: torch.randn(*s, dtype=torch.bfloat16) * 0.2)(F)), 'w2': _w((lambda *s: torch.randn(*s, dtype=torch.bfloat16) * 0.05)(F, D)), 'b2': _w((lambda *s: torch.randn(*s, dtype=torch.bfloat16) * 0.8)(D))}
    _B_in = _B_setup()
    return {'A_x': _A_in['x'], 'A_mem0': _A_in['mem0'], 'A_Wq': _A_in['Wq'], 'A_Wk': _A_in['Wk'], 'A_Wv': _A_in['Wv'], 'A_Wo': _A_in['Wo'], 'A_seg_len': _A_in['seg_len'], 'B_x': _B_in['x'], 'B_w1': _B_in['w1'], 'B_b1': _B_in['b1'], 'B_w2': _B_in['w2'], 'B_b2': _B_in['b2']}

def reference(A_x, A_mem0, A_Wq, A_Wk, A_Wv, A_Wo, A_seg_len, B_x, B_w1, B_b1, B_w2, B_b2):

    def _A_ref(x, mem0, Wq, Wk, Wv, Wo, seg_len):
        B, S, Dm = x.shape
        H, Dh = (Wq.shape[1], Wq.shape[2])
        M = mem0.shape[1]
        scale = Dh ** (-0.5)
        n_seg = S // seg_len
        L = M + seg_len
        idx = torch.arange(L)
        mask = (idx[:, None] < M) | (idx[:, None] >= idx[None, :])
        mem = mem0
        outs = []
        for s in range(n_seg):
            xs = x[:, s * seg_len:(s + 1) * seg_len, :].clone()
            h = torch.concatenate([mem, xs], axis=1)
            q = torch.einsum('btd,dhe->bhte', h, Wq)
            k = torch.einsum('btd,dhe->bhte', h, Wk)
            v = torch.einsum('btd,dhe->bhte', h, Wv)
            scores = torch.einsum('bhqe,bhke->bhqk', q, k) * scale
            scores = torch.where(mask[None, None], scores, -1000000000.0)
            p = _softmax(scores, axis=-1)
            o = torch.einsum('bhqk,bhke->bhqe', p, v)
            o = torch.einsum('bhte,hed->btd', o, Wo)
            mem = o[:, :M, :].clone()
            outs.append(o[:, M:, :])
        return torch.concatenate(outs, axis=1)

    def _B_ref(x, w1, b1, w2, b2):
        h = torch.einsum('bsd,df->bsf', x, w1) + b1
        zero = torch.zeros_like(h)
        r = torch.where(h > zero, h, zero)
        h = r * r
        out = torch.einsum('bsf,fd->bsd', h, w2) + b2
        return out
    x = A_x
    h = x + _A_ref(_layernorm(x, axis=-1), A_mem0, A_Wq, A_Wk, A_Wv, A_Wo, A_seg_len)
    h2 = h + _B_ref(_layernorm(h, axis=-1), B_w1, B_b1, B_w2, B_b2)
    return h2
```

```cpp
#include <hip/hip_runtime.h>
#include <hip/hip_bf16.h>
#include <stdint.h>

typedef __bf16 bf16x8 __attribute__((ext_vector_type(8)));
typedef __bf16 bf16x2 __attribute__((ext_vector_type(2)));
typedef short s16x4 __attribute__((ext_vector_type(4)));
typedef float f32x2 __attribute__((ext_vector_type(2)));
typedef float f32x4 __attribute__((ext_vector_type(4)));
typedef float f32x16 __attribute__((ext_vector_type(16)));
typedef unsigned u32x2 __attribute__((ext_vector_type(2)));
typedef unsigned u32x4 __attribute__((ext_vector_type(4)));
#define LAS __attribute__((address_space(3)))
#define GAS __attribute__((address_space(1)))
#define DI __device__ __forceinline__
typedef __amdgpu_buffer_rsrc_t rsrc_t;

constexpr int DM = 448, NH = 16, DH = 256, SEQ = 2048, NSEG = 64, FF = 1792;

constexpr size_t OFF_CTRL = 0;
constexpr size_t SZ_CTRL = 65536;
constexpr size_t OFF_XNF = OFF_CTRL + SZ_CTRL;
constexpr size_t SZ_SEGF = 28 * 1024;
constexpr size_t OFF_WCT = OFF_XNF + 64 * SZ_SEGF;
constexpr size_t SZ_WCT = (size_t)448 * 56 * 512;
constexpr size_t OFF_KT = OFF_WCT + SZ_WCT;
constexpr size_t SZ_KT = (size_t)64 * 16 * 28672;
constexpr size_t OFF_VT = OFF_KT + SZ_KT;
constexpr size_t OFF_KM = OFF_VT + SZ_KT;
constexpr size_t SZ_KM = (size_t)64 * 16 * 7168;
constexpr size_t OFF_VM = OFF_KM + SZ_KM;
constexpr size_t OFF_OP = OFF_VM + SZ_KM;
constexpr size_t SZ_OP = (size_t)64 * 4 * 57344;
constexpr size_t OFF_HD = OFF_OP + SZ_OP;
constexpr size_t OFF_HNF = OFF_HD + 64 * SZ_SEGF;
constexpr size_t OFF_W1F = OFF_HNF + 64 * SZ_SEGF;
constexpr size_t SZ_W1F = (size_t)56 * 28 * 1024;
constexpr size_t OFF_W2F = OFF_W1F + SZ_W1F;
constexpr size_t OFF_RF = OFF_W2F + SZ_W1F;
constexpr size_t SZ_RF = (size_t)64 * 112 * 1024;
constexpr size_t OFF_END = OFF_RF + SZ_RF;
constexpr size_t SZ_HX = (size_t)4 << 20;

#define CT_TICK(x) (64 * (x))
#define CT_WTICK (64 * 16)
#define CT_TOTAL (64 * 17)
#define CT_ATICK (64 * 18)
#define CT_ABORT (64 * 19)
#define CT_PROBE (64 * 20)
#define CT_KV(s) (64 * (32 + (s)))
#define CT_M(s) (64 * (96 + (s)))
#define CT_A(s) (64 * (160 + (s)))

#define STAMP(wg, idx) do {} while (0)
constexpr int N_REC = 64, N_GEMM = 112, N_WG = 256;
constexpr unsigned SPIN_CAP = 1u << 15;
constexpr float LOG2E = 1.4426950408889634f;

struct Params {
    const uint16_t* x; const uint16_t* mem0; const uint16_t* wq; const uint16_t* wk; const uint16_t* wv; const uint16_t* wo;
    const uint16_t* w1; const uint16_t* b1; const uint16_t* w2; const uint16_t* b2;
    uint16_t* out; char* ws; char* hx;
};

DI float bf_lo(unsigned v) { return __builtin_bit_cast(float, v << 16); }
DI float bf_hi(unsigned v) { return __builtin_bit_cast(float, v & 0xffff0000u); }
DI unsigned pk_bf(float a, float b) { bf16x2 r = {(__bf16)a, (__bf16)b}; return __builtin_bit_cast(unsigned, r); }
DI float rbf(float a) { return bf_lo(pk_bf(a, a)); }
DI float dpp_ror8(float v) { return __builtin_bit_cast(float, __builtin_amdgcn_update_dpp(0, __builtin_bit_cast(int, v), 0x128, 0xf, 0xf, true)); }
DI unsigned fb(float v) { return __builtin_bit_cast(unsigned, v); }
DI float bfl(unsigned v) { return __builtin_bit_cast(float, v); }
DI float xsum32(float v) { auto r = __builtin_amdgcn_permlane32_swap(fb(v), fb(v), false, false); return bfl(r[0]) + bfl(r[1]); }
DI float xsum16(float v) { auto r = __builtin_amdgcn_permlane16_swap(fb(v), fb(v), false, false); return bfl(r[0]) + bfl(r[1]); }
DI float xmax32(float v) { auto r = __builtin_amdgcn_permlane32_swap(fb(v), fb(v), false, false); return fmaxf(bfl(r[0]), bfl(r[1])); }
DI float xmax16(float v) { auto r = __builtin_amdgcn_permlane16_swap(fb(v), fb(v), false, false); return fmaxf(bfl(r[0]), bfl(r[1])); }
DI f32x4 mfma16(u32x4 a, u32x4 b, f32x4 c) { return __builtin_amdgcn_mfma_f32_16x16x32_bf16(__builtin_bit_cast(bf16x8, a), __builtin_bit_cast(bf16x8, b), c, 0, 0, 0); }
DI f32x4 mfma16k16(u32x2 a, u32x2 b, f32x4 c) { return __builtin_amdgcn_mfma_f32_16x16x16bf16_1k(__builtin_bit_cast(s16x4, a), __builtin_bit_cast(s16x4, b), c, 0, 0, 0); }
DI f32x16 mfma32(u32x4 a, u32x4 b, f32x16 c) { return __builtin_amdgcn_mfma_f32_32x32x16_bf16(__builtin_bit_cast(bf16x8, a), __builtin_bit_cast(bf16x8, b), c, 0, 0, 0); }
DI rsrc_t mk_rsrc(const void* p, unsigned bytes) { return __builtin_amdgcn_make_buffer_rsrc((void*)p, 0, bytes, 0x00020000); }
#define LD16(r, vo, so, aux) __builtin_amdgcn_raw_buffer_load_b128((r), (vo), (so), (aux))
#define LD8(r, vo, so, aux) __builtin_amdgcn_raw_buffer_load_b64((r), (vo), (so), (aux))
#define ST16(v, r, vo, so, aux) __builtin_amdgcn_raw_buffer_store_b128((v), (r), (vo), (so), (aux))
#define ST8(v, r, vo, so, aux) __builtin_amdgcn_raw_buffer_store_b64((v), (r), (vo), (so), (aux))
#define ST4(v, r, vo, so, aux) __builtin_amdgcn_raw_buffer_store_b32((v), (r), (vo), (so), (aux))
constexpr int SC1 = 16;
#define BAR() do { asm volatile("s_waitcnt lgkmcnt(0)" ::: "memory"); __builtin_amdgcn_s_barrier(); asm volatile("" ::: "memory"); } while (0)
#define GLDS(src, dst, aux) __builtin_amdgcn_global_load_lds((const GAS void*)(src), (LAS void*)(dst), 16, 0, (aux))
DI unsigned ld_agent(const unsigned* p) { return __hip_atomic_load((const GAS unsigned*)p, __ATOMIC_RELAXED, __HIP_MEMORY_SCOPE_AGENT); }
DI void st_agent(unsigned* p, unsigned v) { __hip_atomic_store((GAS unsigned*)p, v, __ATOMIC_RELAXED, __HIP_MEMORY_SCOPE_AGENT); }
DI unsigned add_agent(unsigned* p, unsigned v) { return __hip_atomic_fetch_add((GAS unsigned*)p, v, __ATOMIC_RELAXED, __HIP_MEMORY_SCOPE_AGENT); }
DI int tid_opaque() { int t = threadIdx.x; asm volatile("" : "+v"(t)); return t; }
DI unsigned xcc_id() { return (unsigned)__builtin_amdgcn_s_getreg(20 | (3 << 11)) & 0xfu; }
DI float wave_sum(float v) {
    v += dpp_ror8(v);
    v += __builtin_bit_cast(float, __builtin_amdgcn_update_dpp(0, __builtin_bit_cast(int, v), 0x124, 0xf, 0xf, true));
    v += __builtin_bit_cast(float, __builtin_amdgcn_update_dpp(0, __builtin_bit_cast(int, v), 0x122, 0xf, 0xf, true));
    v += __builtin_bit_cast(float, __builtin_amdgcn_update_dpp(0, __builtin_bit_cast(int, v), 0x121, 0xf, 0xf, true));
    return xsum32(xsum16(v));
}
DI void wait_count(unsigned* ctrl, int word, unsigned target) {
    for (unsigned sp = 0; sp < SPIN_CAP; ++sp) {
        unsigned v = __builtin_amdgcn_readfirstlane(ld_agent(ctrl + word));
        if (v >= target) return;
        __builtin_amdgcn_s_sleep(4);
        if ((sp & 255u) == 255u && __builtin_amdgcn_readfirstlane(ld_agent(ctrl + CT_ABORT))) return;
    }
    st_agent(ctrl + CT_ABORT, 1u);
}

constexpr int K1_AB = 512, K1_LN = 128, K1_W1 = 56, K1_W2 = 56, K1_PZ = 64;
constexpr int K1_GRID = K1_AB + K1_LN + K1_W1 + K1_W2 + K1_PZ + 1;
constexpr int K1_LDS = 2 * 112 * 272 + 1024;

__global__ void __launch_bounds__(256, 2) k1_prep(Params p) {
    extern __shared__ __attribute__((aligned(16))) char smem[];
    const int b = blockIdx.x, tid = threadIdx.x, lane = tid & 63, w = tid >> 6;
    if (b < K1_AB) {
        const int m = b >> 4, tr = (b >> 2) & 3, tc = b & 3, hd = m & 15;
        const bool isA = m < 16;
        const uint16_t* arow = (isA ? p.wk : p.wv) + (size_t)(112 * tr) * 4096 + hd * 256;
        const uint16_t* brow = p.wq + (size_t)(112 * tc) * 4096 + hd * 256;
        const uint16_t* bcol = p.wo + (size_t)hd * 256 * 448 + 112 * tc;
        char* As = smem; char* Bs = smem + 112 * 272;
        u32x4 ra[2][7], rb[2][7];
#pragma unroll
        for (int kh = 0; kh < 2; ++kh)
#pragma unroll
            for (int i = 0; i < 7; ++i) {
                const int ch = tid + 256 * i;
                ra[kh][i] = *(const u32x4*)(arow + (size_t)(ch >> 4) * 4096 + kh * 128 + (ch & 15) * 8);
                if (isA) rb[kh][i] = *(const u32x4*)(brow + (size_t)(ch >> 4) * 4096 + kh * 128 + (ch & 15) * 8);
                else     rb[kh][i] = *(const u32x4*)(bcol + (size_t)(kh * 128 + ch / 14) * 448 + (ch % 14) * 8);
            }
        f32x4 acc[2][7];
#pragma unroll
        for (int i = 0; i < 2; ++i)
#pragma unroll
            for (int j = 0; j < 7; ++j) acc[i][j] = (f32x4){0.f, 0.f, 0.f, 0.f};
        const int x = lane & 15, q = lane >> 4;
#pragma unroll
        for (int kh = 0; kh < 2; ++kh) {
            if (kh) __syncthreads();
#pragma unroll
            for (int i = 0; i < 7; ++i) {
                const int ch = tid + 256 * i;
                *(u32x4*)(As + (ch >> 4) * 272 + (ch & 15) * 16) = ra[kh][i];
                if (isA) *(u32x4*)(Bs + (ch >> 4) * 272 + (ch & 15) * 16) = rb[kh][i];
                else     *(u32x4*)(Bs + (ch / 14) * 240 + (ch % 14) * 16) = rb[kh][i];
            }
            __syncthreads();
#pragma unroll
            for (int ks = 0; ks < 4; ++ks) {
                u32x4 bf[7];
#pragma unroll
                for (int j = 0; j < 7; ++j) {
                    if (isA) bf[j] = *(const u32x4*)(Bs + (16 * j + x) * 272 + ks * 64 + q * 16);
                    else {
                        const int r0 = 32 * ks + 8 * q + (x >> 2), cb = (16 * j + 4 * (x & 3)) * 2;
                        s16x4 lo = __builtin_amdgcn_ds_read_tr16_b64_v4i16((LAS s16x4*)(Bs + r0 * 240 + cb));
                        s16x4 hi = __builtin_amdgcn_ds_read_tr16_b64_v4i16((LAS s16x4*)(Bs + (r0 + 4) * 240 + cb));
                        u32x2 l2 = __builtin_bit_cast(u32x2, lo), h2 = __builtin_bit_cast(u32x2, hi);
                        bf[j] = (u32x4){l2.x, l2.y, h2.x, h2.y};
                    }
                }
#pragma unroll
                for (int i = 0; i < 2; ++i) {
                    const int rt = w + 4 * i;
                    if (rt < 7) {
                        const u32x4 af = *(const u32x4*)(As + (16 * rt + x) * 272 + ks * 64 + q * 16);
#pragma unroll
                        for (int j = 0; j < 7; ++j) acc[i][j] = mfma16(af, bf[j], acc[i][j]);
                    }
                }
            }
        }
        const float sc = isA ? 0.0625f : 1.0f;
        char* wct = p.ws + OFF_WCT;
#pragma unroll
        for (int i = 0; i < 2; ++i) {
            const int rt = w + 4 * i;
            if (rt < 7) {
#pragma unroll
                for (int j = 0; j < 7; ++j) {
                    const int np = (isA ? 0 : 7168) + hd * 448 + 112 * tc + 16 * j + x;
                    const int k = 112 * tr + 16 * rt + 4 * q;
                    u32x2 v = {pk_bf(acc[i][j][0] * sc, acc[i][j][1] * sc), pk_bf(acc[i][j][2] * sc, acc[i][j][3] * sc)};
                    *(u32x2*)(wct + ((size_t)((np >> 5) * 56 + (k >> 3)) * 32 + (np & 31)) * 16 + ((k >> 2) & 1) * 8) = v;
                }
            }
        }
        return;
    }
    int bb = b - K1_AB;
    if (bb < K1_LN) {
#pragma unroll
        for (int i = 0; i < 4; ++i) {
            const int row = bb * 16 + w * 4 + i;
            float v[8];
            u32x4 raw = {0u, 0u, 0u, 0u};
            if (lane < 56) raw = *(const u32x4*)(p.x + (size_t)row * DM + lane * 8);
            v[0] = bf_lo(raw.x); v[1] = bf_hi(raw.x); v[2] = bf_lo(raw.y); v[3] = bf_hi(raw.y);
            v[4] = bf_lo(raw.z); v[5] = bf_hi(raw.z); v[6] = bf_lo(raw.w); v[7] = bf_hi(raw.w);
            float s = 0.f;
#pragma unroll
            for (int e = 0; e < 8; ++e) s += v[e];
            const float mean = wave_sum(s) * (1.0f / DM);
            float s2 = 0.f;
            if (lane < 56) {
#pragma unroll
                for (int e = 0; e < 8; ++e) { const float d = v[e] - mean; s2 += d * d; }
            }
            const float var = wave_sum(s2) * (1.0f / DM);
            const float mb = rbf(mean);
            const float den = rbf(sqrtf(rbf(rbf(var) + 1e-5f)));
            if (lane < 56) {
                float y[8];
#pragma unroll
                for (int e = 0; e < 8; ++e) y[e] = rbf(v[e] - mb) / den;
                u32x4 o = {pk_bf(y[0], y[1]), pk_bf(y[2], y[3]), pk_bf(y[4], y[5]), pk_bf(y[6], y[7])};
                *(u32x4*)(p.ws + OFF_XNF + (size_t)(row >> 5) * SZ_SEGF + ((lane >> 1) * 64 + (lane & 1) * 32 + (row & 31)) * 16) = o;
            }
        }
        return;
    }
    bb -= K1_LN;
    if (bb < K1_W1 + K1_W2) {
        const bool is1 = bb < K1_W1;
        const uint16_t* src; int stride; char* dst;
        if (is1) { src = p.w1 + 32 * bb; stride = FF; dst = p.ws + OFF_W1F + (size_t)bb * 28 * 1024; }
        else { const int t = bb - K1_W1, ct = t >> 2, fq = t & 3; src = p.w2 + (size_t)(448 * fq) * DM + 32 * ct; stride = DM; dst = p.ws + OFF_W2F + ((size_t)ct * 112 + 28 * fq) * 1024; }
#pragma unroll
        for (int i = 0; i < 7; ++i) {
            const int ch = tid + 256 * i;
            *(u32x4*)(smem + (ch >> 2) * 80 + (ch & 3) * 16) = *(const u32x4*)(src + (size_t)(ch >> 2) * stride + (ch & 3) * 8);
        }
        __syncthreads();
        const int hh = lane >> 5, blk = (lane >> 4) & 1, a = (lane & 15) >> 2, pp = lane & 3;
#pragma unroll
        for (int i = 0; i < 7; ++i) {
            const int ks = w + 4 * i;
            const int r0 = 16 * ks + 4 * hh + a, cb = (16 * blk + 4 * pp) * 2;
            s16x4 lo = __builtin_amdgcn_ds_read_tr16_b64_v4i16((LAS s16x4*)(smem + r0 * 80 + cb));
            s16x4 hi = __builtin_amdgcn_ds_read_tr16_b64_v4i16((LAS s16x4*)(smem + (r0 + 8) * 80 + cb));
            u32x2 l2 = __builtin_bit_cast(u32x2, lo), h2 = __builtin_bit_cast(u32x2, hi);
            *(u32x4*)(dst + (size_t)ks * 1024 + lane * 16) = (u32x4){l2.x, l2.y, h2.x, h2.y};
        }
        return;
    }
    bb -= K1_W1 + K1_W2;
    if (bb < K1_PZ) {
        const u32x4 ones = {~0u, ~0u, ~0u, ~0u};
#pragma unroll
        for (int i = 0; i < 16; ++i) *(u32x4*)(p.hx + ((size_t)bb * 4096 + i * 256 + tid) * 16) = ones;
        return;
    }
    {
        const u32x4 z = {0u, 0u, 0u, 0u};
#pragma unroll
        for (int i = 0; i < 16; ++i) *(u32x4*)(p.ws + OFF_CTRL + ((size_t)i * 256 + tid) * 16) = z;
    }
}

constexpr int GEMM_LDS = 2 * 2 * 28 * 1024;

template <int MODE>
DI void gemm448(const Params& p, char* smem, int slice, int seg0, int npair) {
    const int tid = threadIdx.x, lane = tid & 63, w = __builtin_amdgcn_readfirstlane(tid >> 6);
    const int ct = w & 3, th = w >> 2, T = 4 * slice + ct;
    unsigned* ctrl = (unsigned*)(p.ws + OFF_CTRL);
    const char* src = p.ws + (MODE == 0 ? OFF_XNF : OFF_HNF) + (size_t)seg0 * SZ_SEGF;
    u32x4 wf[28];
    if (MODE == 0) {
        const char* wb = p.ws + OFF_WCT + (size_t)T * 28672 + lane * 16;
#pragma unroll
        for (int ks = 0; ks < 28; ++ks) wf[ks] = *(const u32x4*)(wb + ks * 1024);
    } else {
        const char* wb = p.ws + OFF_W1F + (size_t)T * 28672 + lane * 16;
#pragma unroll
        for (int ks = 0; ks < 28; ++ks) wf[ks] = *(const u32x4*)(wb + ks * 1024);
    }
#pragma unroll
    for (int pr = 0; pr < 2; ++pr)
        if (pr < npair) {
#pragma unroll
            for (int i = 0; i < 7; ++i) GLDS(src + (size_t)pr * 57344 + (w * 7 + i) * 1024 + lane * 16, smem + pr * 57344 + (w * 7 + i) * 1024, 0);
        }
    asm volatile("s_waitcnt vmcnt(0)" ::: "memory");
    if (MODE == 0 && w == 0) STAMP(64 + slice, 1);
    const bool isK = (MODE == 0) && (T < 224);
    const int Th = isK ? T : T - 224, hd = Th / 14, ctile = Th % 14;
    const rsrc_t ro = (MODE == 0) ? mk_rsrc(p.ws + (isK ? OFF_KT : OFF_VT), (unsigned)SZ_KT) : mk_rsrc(p.ws + OFF_RF, (unsigned)SZ_RF);
    u32x2 bias[4];
    if (MODE == 1) {
#pragma unroll
        for (int g = 0; g < 4; ++g) bias[g] = *(const u32x2*)(p.b1 + 32 * T + 8 * g + 4 * (lane >> 5));
    }
    for (int pr = 0; pr < npair; ++pr) {
        if (pr >= 1) {
            if (pr + 1 < npair) asm volatile("s_waitcnt vmcnt(7)" ::: "memory"); else asm volatile("s_waitcnt vmcnt(0)" ::: "memory");
        }
        BAR();
        if (MODE == 0 && w == 0) STAMP(64 + slice, 2 + pr);
        if (MODE == 0 && pr >= 1 && tid_opaque() < 2) add_agent(ctrl + CT_KV(seg0 + 2 * (pr - 1) + tid), 1u);
        const char* xs = smem + (pr & 1) * 57344 + th * 28672 + lane * 16;
        f32x16 acc;
#pragma unroll
        for (int i = 0; i < 16; ++i) acc[i] = 0.f;
        if (MODE == 0 && !isK) {
#pragma unroll
            for (int ks = 0; ks < 28; ++ks) acc = mfma32(*(const u32x4*)(xs + ks * 1024), wf[ks], acc);
        } else {
#pragma unroll
            for (int ks = 0; ks < 28; ++ks) acc = mfma32(wf[ks], *(const u32x4*)(xs + ks * 1024), acc);
        }
        const int s = seg0 + 2 * pr + th;
        if (MODE == 0) {
            unsigned pk[8];
#pragma unroll
            for (int i = 0; i < 8; ++i) pk[i] = pk_bf(acc[2 * i], acc[2 * i + 1]);
            if (isK) {
#pragma unroll
                for (int k = 0; k < 2; ++k) {
                    auto r0 = __builtin_amdgcn_permlane32_swap(pk[4 * k], pk[4 * k + 2], false, false);
                    auto r1 = __builtin_amdgcn_permlane32_swap(pk[4 * k + 1], pk[4 * k + 3], false, false);
                    u32x4 v = {r0[0], r1[0], r0[1], r1[1]};
                    ST16(v, ro, lane * 16 + k * 1024, ((s * 16 + hd) * 56 + 4 * ctile) * 512, SC1);
                }
            } else {
#pragma unroll
                for (int k = 0; k < 2; ++k) {
                    u32x4 v = {pk[4 * k], pk[4 * k + 1], pk[4 * k + 2], pk[4 * k + 3]};
                    ST16(v, ro, lane * 16 + k * 1024, ((s * 16 + hd) * 14 + ctile) * 2048, SC1);
                }
            }
        } else {
            unsigned pk[8];
#pragma unroll
            for (int g = 0; g < 4; ++g) {
                float bv[4] = {bf_lo(bias[g].x), bf_hi(bias[g].x), bf_lo(bias[g].y), bf_hi(bias[g].y)};
                float r[4];
#pragma unroll
                for (int e = 0; e < 4; ++e) { float t = rbf(rbf(acc[4 * g + e]) + bv[e]); t = t > 0.f ? t : 0.f; r[e] = t * t; }
                pk[2 * g] = pk_bf(r[0], r[1]); pk[2 * g + 1] = pk_bf(r[2], r[3]);
            }
#pragma unroll
            for (int k = 0; k < 2; ++k) {
                u32x4 v = {pk[4 * k], pk[4 * k + 1], pk[4 * k + 2], pk[4 * k + 3]};
                ST16(v, ro, lane * 16 + k * 1024, (s * 112 + 2 * T) * 1024, 0);
            }
        }
        if (pr + 2 < npair) {
            BAR();
#pragma unroll
            for (int i = 0; i < 7; ++i) GLDS(src + (size_t)(pr + 2) * 57344 + (w * 7 + i) * 1024 + lane * 16, smem + (pr & 1) * 57344 + (w * 7 + i) * 1024, 0);
        }
    }
    if (MODE == 0) {
        asm volatile("s_waitcnt vmcnt(0)" ::: "memory");
        BAR();
        if (tid_opaque() < 2) add_agent(ctrl + CT_KV(seg0 + 2 * (npair - 1) + tid), 1u);
    }
}

constexpr int L_F = 0;
constexpr int L_R = 14336;
constexpr int L_MX = L_R + 1792;
constexpr int L_R2 = L_MX + 128;

constexpr unsigned POLL_CAP = 1u << 14;
DI u32x4 poll16(rsrc_t r, int vo, int so, unsigned* ctrl, unsigned& cap, unsigned code = 1u) {
    u32x4 v;
    unsigned sp = 0;
    bool bad;
    do {
        asm volatile("" ::: "memory");
        v = LD16(r, vo, so, SC1);
        bad = __any(max(max(v.x, v.y), max(v.z, v.w)) == 0xffffffffu);
    } while (bad && ++sp < cap);
    if (bad) { st_agent(ctrl + CT_ABORT, 1u); cap = 1u; }
    return v;
}
DI float fmax_raw(float a, float b) { float r; asm("v_max_f32 %0, %1, %2" : "=v"(r) : "v"(a), "v"(b)); return r; }
DI float fmax3_raw(float a, float b, float c) { float r; asm("v_max3_f32 %0, %1, %2, %3" : "=v"(r) : "v"(a), "v"(b), "v"(c)); return r; }

#define DELAY_X 7

template <bool BROLE, bool W7>
DI void rec_wave(const Params& p, char* smem, const int c, const int w) {
    const int tid = threadIdx.x, lane = tid & 63;
    const int x = lane & 15, q = lane >> 4, ap = (lane >> 3) & 1, i8 = lane & 7;
    const int h = c >> 2, c4 = c & 3;
    const int u = c >= 28 ? 1 : 0, J = c - 28 * u, g = 8 * u + w;
    unsigned* ctrl = (unsigned*)(p.ws + OFF_CTRL);
    const rsrc_t rhx = mk_rsrc(p.hx, (unsigned)SZ_HX), rkt = mk_rsrc(p.ws + OFF_KT, (unsigned)SZ_KT), rvt = mk_rsrc(p.ws + OFF_VT, (unsigned)SZ_KT);
    const rsrc_t rkm = mk_rsrc(p.ws + OFF_KM, (unsigned)SZ_KM), rvm = mk_rsrc(p.ws + OFF_VM, (unsigned)SZ_KM);
    unsigned cap = POLL_CAP;

    u32x4 wA[14], wB[14];
    auto load_w = [&](u32x4 (&wt)[14], int np) {
        const char* b = p.ws + OFF_WCT + ((size_t)(np >> 5) * 56 * 32 + q * 32 + (np & 31)) * 16;
#pragma unroll
        for (int ks = 0; ks < 14; ++ks) wt[ks] = *(const u32x4*)(b + ks * 2048);
    };
    if (!W7) load_w(wA, h * 448 + 112 * c4 + 16 * w + x);
    if (BROLE) load_w(wB, 7168 + g * 448 + 16 * J + x);
    if (!W7 && c == 0 && w == 0) {
        unsigned res = 0x100u;
        for (int ch = 0; ch < 2; ++ch) {
            unsigned best0 = ~0u, best1 = ~0u;
            for (int rep = 0; rep < 6; ++rep) {
                const int off = ch * (2 << 20) + 16384 + (rep & 1) * 8192 + 6144;
                const unsigned long long t0 = __builtin_amdgcn_s_memtime();
                ST4((unsigned)rep, rhx, 0, off, SC1);
                asm volatile("s_waitcnt vmcnt(0)" ::: "memory");
                unsigned v = __builtin_amdgcn_raw_buffer_load_b32(rhx, 0, off, SC1);
                asm volatile("s_waitcnt vmcnt(0)" : "+v"(v) :: "memory");
                const unsigned dt = (unsigned)(__builtin_amdgcn_s_memtime() - t0);
                if (rep & 1) best1 = min(best1, dt); else best0 = min(best0, dt);
            }
            res |= (best1 < best0 ? 1u : 0u) << ch;
        }
        if (lane == 0) st_agent(ctrl + CT_PROBE, res);
    }
    if (!W7) {
        const int o = tid >> 3, i = tid & 7;
        *(u32x4*)(smem + L_F + (o * 16 + i) * 16) = *(const u32x4*)(p.mem0 + i * DM + o * 8);
        *(u32x4*)(smem + L_F + (o * 16 + 8 + i) * 16) = (u32x4){0u, 0u, 0u, 0u};
    }
    wait_count(ctrl, CT_PROBE, 1u);
    const unsigned probe = __builtin_amdgcn_readfirstlane(ld_agent(ctrl + CT_PROBE));
    wait_count(ctrl, CT_KV(0), N_GEMM);
    wait_count(ctrl, CT_KV(1), N_GEMM);

    STAMP(c, w);
    auto phys = [](int lo) { return ((lo >> 13) << 14) + (lo & 8191); };
    const int vo_x0 = phys(tid * 16), vo_x1 = phys(7168 + tid * 16);
    const int vo_yl = phys(16384 + (g * 4 + 2 * ap + (q >> 1)) * 256 + i8 * 32 + (q & 1) * 16);
    const int vo_ys = phys(16384 + c * 256 + lane * 4);
    const int vo_xs = phys(u * 7168 + J * 256 + lane * 4);
    const int vo_k = (q * 32 + 8 * c4 + i8) * 16;
    const int vo_v = ((q & 1) * 32 + 16 * (J & 1) + x) * 16 + 8 * (q >> 1);
    const int vo_km = ((14 * c4 + 2 * w + (q >> 1)) * 8 + i8) * 16 + (q & 1) * 8;
    const int vo_vm = (16 * J + x) * 16 + (q & 1) * 8;
    const int scol = 112 * c4 + 16 * w + 4 * q;
    const int lo_s = L_F + ((scol >> 3) * 16 + x) * 16 + (scol & 4) * 2;
    const int lo_f = L_F + ((tid >> 3) * 16 + (tid & 7)) * 16;
    auto step_off = [&](int s) { return (s >> 5) * (2 << 20) + (s & 31) * 65536 + ((((probe >> (s >> 5)) ^ (s >> 4)) & 1) << 13); };

    u32x2 v1 = {0u, 0u}, v2 = {0u, 0u};
    auto v_load = [&](int s) {
        const int so = ((s * 16 + g) * 14 + (J >> 1)) * 2048;
        v1 = LD8(rvt, vo_v, so, SC1); v2 = LD8(rvt, vo_v + 1024, so, SC1);
    };
    auto k_load = [&](int s) {
#pragma unroll
        for (int ks = 0; ks < 14; ++ks) wA[ks] = LD16(rkt, vo_k, (s * 16 + h) * 28672 + ks * 2048, SC1);
    };
    if (BROLE) v_load(0);
    if (W7) k_load(0);
    u32x4 fr[14];
    auto gather_x = [&](int s) {
        const int so_s = step_off(s);
#pragma unroll
        for (int i = 0; i < DELAY_X; ++i) __builtin_amdgcn_s_sleep(1);
        asm volatile("" ::: "memory");
        u32x4 a0 = LD16(rhx, vo_x0, so_s, SC1);
        const u32x4 a1 = poll16(rhx, vo_x1, so_s, ctrl, cap, 0x1000000u | (s << 16) | (c << 8) | w);
        if (__any(max(max(a0.x, a0.y), max(a0.z, a0.w)) == 0xffffffffu)) a0 = poll16(rhx, vo_x0, so_s, ctrl, cap, 0x2000000u | (s << 16) | (c << 8) | w);
        if (w == 1) STAMP(c, 8 + (s - 1) * 8 + 4);
        *(u32x4*)(smem + lo_f) = a0;
        *(u32x4*)(smem + lo_f + 128) = a1;
    };
    auto a_mfma = [&]() {
#pragma unroll
        for (int ks = 0; ks < 14; ++ks) fr[ks] = *(const u32x4*)(smem + L_F + ks * 1024 + lane * 16);
        f32x4 acc = {0.f, 0.f, 0.f, 0.f};
#pragma unroll
        for (int ks = 0; ks < 14; ++ks) acc = mfma16(wA[ks], fr[ks], acc);
#pragma unroll
        for (int r = 0; r < 4; ++r) acc[r] += dpp_ror8(acc[r]);
        return (u32x2){pk_bf(acc[0], acc[1]), pk_bf(acc[2], acc[3])};
    };
    auto u_part = [&]() {
        f32x4 ua = {0.f, 0.f, 0.f, 0.f};
#pragma unroll
        for (int ks = 0; ks < 14; ++ks) ua = mfma16(fr[ks], wB[ks], ua);
#pragma unroll
        for (int r = 0; r < 4; ++r) ua[r] = xsum32(ua[r]);
        return (u32x2){pk_bf(ua[0], ua[1]), pk_bf(ua[2], ua[3])};
    };

    for (int s = 0; s < NSEG - 1; ++s) {
        const int so_s = step_off(s);
        BAR();
        if (w == 0) STAMP(c, 8 + s * 8 + 0);
        if (!W7 && s > 0 && tid_opaque() == 0) add_agent(ctrl + CT_M(s - 1), 1u);
        if (!W7) {
            const u32x2 sop = *(const u32x2*)(smem + lo_s);
            const u32x2 tb = a_mfma();
            f32x4 st = mfma16k16(tb, sop, (f32x4){0.f, 0.f, 0.f, 0.f});
#pragma unroll
            for (int r = 0; r < 4; ++r) st[r] += dpp_ror8(st[r]);
            if (x < 8 && q < 2) *(f32x4*)(smem + L_R + (w * 16 + 2 * x + q) * 16) = st;
            if (x < 8) ST8(tb, rkm, vo_km, (s * 16 + h) * 7168, SC1);
        } else {
            const u32x2 tb = a_mfma();
            if (x < 8 && q < 2) *(u32x2*)(smem + L_MX + (2 * x + q) * 8) = tb;
        }
        BAR();
        if (w == 0) STAMP(c, 8 + s * 8 + 1);
        if (!W7 && w == 0) {
            const int chunk = lane >> 2, part = lane & 3;
            f32x2 t[7];
#pragma unroll
            for (int i = 0; i < 7; ++i) t[i] = *(const f32x2*)(smem + L_R + (i * 16 + chunk) * 16 + (part & 1) * 8);
            const unsigned mxv = *(const unsigned*)(smem + L_MX + chunk * 8 + (part & 1) * 4);
            const f32x2 sum = ((t[0] + t[1]) + (t[2] + t[3])) + ((t[4] + t[5]) + t[6]);
            unsigned v = part < 2 ? mxv : pk_bf(sum[0], sum[1]);
            ST4(min(v, 0xfffffffeu), rhx, vo_ys, so_s, SC1);
        }
        if (W7) k_load(s + 1);
        if (BROLE) {
            const u32x2 ub = u_part();
            const u32x4 y = poll16(rhx, vo_yl, so_s, ctrl, cap, 0x3000000u | (s << 16) | (c << 8) | w);
            if (w == 1) STAMP(c, 8 + s * 8 + 2);
            if (q < 2) ST8(ub, rvm, vo_vm, (s * 16 + g) * 7168, SC1);
            const float x0 = bf_lo(y.x), x1 = bf_hi(y.x), x2 = bf_lo(y.y), x3 = bf_hi(y.y);
            auto s01 = __builtin_amdgcn_permlane32_swap(y.z << 16, y.z & 0xffff0000u, false, false);
            auto s23 = __builtin_amdgcn_permlane32_swap(y.w << 16, y.w & 0xffff0000u, false, false);
            const float w0 = bfl(s01[0]) + bfl(s01[1]), w1 = bfl(s23[0]) + bfl(s23[1]);
            const float keep = ap ? w1 : w0, give = ap ? w0 : w1;
            const float sm = keep + dpp_ror8(give);
            float mx = fmax3_raw(fmax3_raw(x0, x1, x2), x3, sm);
            mx = fmax_raw(mx, dpp_ror8(mx));
            { auto r = __builtin_amdgcn_permlane16_swap(fb(mx), fb(mx), false, false); mx = fmax_raw(bfl(r[0]), bfl(r[1])); }
            { auto r = __builtin_amdgcn_permlane32_swap(fb(mx), fb(mx), false, false); mx = fmax_raw(bfl(r[0]), bfl(r[1])); }
            const float mc = mx * LOG2E;
            const float e0 = __builtin_amdgcn_exp2f(x0 * LOG2E - mc), e1 = __builtin_amdgcn_exp2f(x1 * LOG2E - mc);
            const float e2 = __builtin_amdgcn_exp2f(x2 * LOG2E - mc), e3 = __builtin_amdgcn_exp2f(x3 * LOG2E - mc);
            const float em = __builtin_amdgcn_exp2f(sm * LOG2E - mc);
            const unsigned b01 = pk_bf(e0, e1), b23 = pk_bf(e2, e3);
            const unsigned bm = (q >> 1) ? pk_bf(0.f, em) : pk_bf(em, 0.f);
            const u32x2 pm = {ap ? 0u : bm, ap ? bm : 0u};
            const u32x2 p1 = {ap ? 0u : b01, ap ? 0u : b23}, p2 = {ap ? b01 : 0u, ap ? b23 : 0u};
            f32x4 o = mfma16k16(ub, pm, (f32x4){0.f, 0.f, 0.f, 0.f});
            o = mfma16k16(v1, p1, o);
            o = mfma16k16(v2, p2, o);
            float l = ((e0 + e1) + (e2 + e3)) + em;
            l += dpp_ror8(l);
            l = xsum32(xsum16(l));
            const float inv = __builtin_amdgcn_rcpf(l);
#pragma unroll
            for (int r = 0; r < 4; ++r) { o[r] *= inv; o[r] += dpp_ror8(o[r]); }
            if (x < 8) *(f32x4*)(smem + L_R2 + ((w * 8 + x) * 4 + q) * 16) = o;
            BAR();
            if (w == 0) STAMP(c, 8 + s * 8 + 3);
            if (W7) {
                const int hf = lane >> 5, i = (lane >> 2) & 7, d = lane & 3;
                f32x2 t[8];
#pragma unroll
                for (int k = 0; k < 8; ++k) t[k] = *(const f32x2*)(smem + L_R2 + ((k * 8 + i) * 4 + 2 * hf + (d >> 1)) * 16 + (d & 1) * 8);
                const f32x2 sum = ((t[0] + t[1]) + (t[2] + t[3])) + ((t[4] + t[5]) + (t[6] + t[7]));
                ST4(min(pk_bf(sum[0], sum[1]), 0xfffffffeu), rhx, vo_xs, step_off(s + 1), SC1);
            }
            v_load(s + 1);
        }
        if (W7) {
            if (s + 2 < NSEG) wait_count(ctrl, CT_KV(s + 2), N_GEMM);
            asm volatile("s_waitcnt vmcnt(0)" ::: "memory");
        } else {
            gather_x(s + 1);
        }
    }
    {
        const int s = NSEG - 1;
        BAR();
        if (!W7 && tid_opaque() == 0) add_agent(ctrl + CT_M(s - 1), 1u);
        if (!W7) {
            const u32x2 tb = a_mfma();
            if (x < 8) ST8(tb, rkm, vo_km, (s * 16 + h) * 7168, SC1);
        } else {
#pragma unroll
            for (int ks = 0; ks < 14; ++ks) fr[ks] = *(const u32x4*)(smem + L_F + ks * 1024 + lane * 16);
        }
        if (BROLE) { const u32x2 ub = u_part(); if (q < 2) ST8(ub, rvm, vo_vm, (s * 16 + g) * 7168, SC1); }
        asm volatile("s_waitcnt vmcnt(0)" ::: "memory");
        BAR();
        if (!W7 && tid_opaque() == 0) add_agent(ctrl + CT_M(s), 1u);
    }
}

DI void rec_role(const Params& p, char* smem, int c) {
    const int w = __builtin_amdgcn_readfirstlane(threadIdx.x >> 6);
    if (c < 56) { if (w < 7) rec_wave<true, false>(p, smem, c, w); else rec_wave<true, true>(p, smem, c, w); }
    else        { if (w < 7) rec_wave<false, false>(p, smem, c, w); else rec_wave<false, true>(p, smem, c, w); }
}

constexpr int LA_X = 0;
constexpr int LA_SM = 28672;
constexpr int LA_PX = LA_SM + 16384;
constexpr int LA_PM = LA_PX + 8192;
constexpr int LA_RED = LA_PM + 2048;
constexpr int LA_BC = LA_RED + 1024;

DI void attn_item(const Params& p, char* smem, int s, int hg) {
    const int tid = threadIdx.x, lane = tid & 63, w = __builtin_amdgcn_readfirstlane(tid >> 6);
    const int tok = lane & 31, hh = lane >> 5;
    unsigned* ctrl = (unsigned*)(p.ws + OFF_CTRL);
    const rsrc_t rkt = mk_rsrc(p.ws + OFF_KT, (unsigned)SZ_KT), rvt = mk_rsrc(p.ws + OFF_VT, (unsigned)SZ_KT);
    const rsrc_t rkm = mk_rsrc(p.ws + OFF_KM, (unsigned)SZ_KM), rvm = mk_rsrc(p.ws + OFF_VM, (unsigned)SZ_KM);
    const rsrc_t rop = mk_rsrc(p.ws + OFF_OP, (unsigned)SZ_OP);
    {
        const char* src = p.ws + OFF_XNF + (size_t)s * SZ_SEGF;
#pragma unroll
        for (int i = 0; i < 4; ++i) { const int k = w + 8 * i; if (k < 28) GLDS(src + k * 1024 + lane * 16, smem + LA_X + k * 1024, 0); }
    }
    if (w == 0) wait_count(ctrl, CT_KV(s), N_GEMM);
    asm volatile("s_waitcnt vmcnt(0)" ::: "memory");
    BAR();
    const int nt = w < 6 ? 2 : 1;
    f32x16 sx;
#pragma unroll
    for (int i = 0; i < 16; ++i) sx[i] = 0.f;
    if (w < 4) {
        u32x4 kf[28];
#pragma unroll
        for (int ks = 0; ks < 28; ++ks) kf[ks] = LD16(rkt, lane * 16, (s * 16 + 4 * hg + w) * 28672 + ks * 1024, SC1);
#pragma unroll
        for (int ks = 0; ks < 28; ++ks) sx = mfma32(kf[ks], *(const u32x4*)(smem + LA_X + ks * 1024 + lane * 16), sx);
    }
    u32x4 vf[2][8];
#pragma unroll
    for (int t = 0; t < 2; ++t)
        if (t < nt) {
#pragma unroll
            for (int k = 0; k < 8; ++k) vf[t][k] = LD16(rvt, lane * 16, (((s * 16 + 4 * hg + (k >> 1)) * 14 + w + 8 * t) * 2 + (k & 1)) * 1024, SC1);
        }
    if (w == 4) wait_count(ctrl, CT_M(s), N_REC);
    BAR();
    u32x2 vm[2][4];
#pragma unroll
    for (int t = 0; t < 2; ++t)
        if (t < nt) {
#pragma unroll
            for (int k = 0; k < 4; ++k) vm[t][k] = LD8(rvm, (32 * (w + 8 * t) + tok) * 16 + hh * 8, (s * 16 + 4 * hg + k) * 7168, SC1);
        }
    if (w >= 4) {
        const int kq = w - 4;
        u32x4 mf[7];
#pragma unroll
        for (int i = 0; i < 7; ++i) mf[i] = LD16(rkm, (hh * 8 + (tok & 7)) * 16 + (tok >> 3) * 7168, (s * 16 + 4 * hg) * 7168 + (7 * kq + i) * 256, SC1);
        f32x16 sm;
#pragma unroll
        for (int i = 0; i < 16; ++i) sm[i] = 0.f;
#pragma unroll
        for (int i = 0; i < 7; ++i) sm = mfma32(mf[i], *(const u32x4*)(smem + LA_X + (7 * kq + i) * 1024 + lane * 16), sm);
#pragma unroll
        for (int gq = 0; gq < 4; ++gq) *(f32x4*)(smem + LA_SM + ((kq * 4 + gq) * 64 + lane) * 16) = (f32x4){sm[4 * gq], sm[4 * gq + 1], sm[4 * gq + 2], sm[4 * gq + 3]};
    }
    BAR();
    if (w < 4) {
        f32x4 m4 = *(const f32x4*)(smem + LA_SM + ((0 * 4 + w) * 64 + lane) * 16);
#pragma unroll
        for (int kq = 1; kq < 4; ++kq) m4 += *(const f32x4*)(smem + LA_SM + ((kq * 4 + w) * 64 + lane) * 16);
        float mx = fmaxf(fmaxf(m4[0], m4[1]), fmaxf(m4[2], m4[3]));
#pragma unroll
        for (int i = 0; i < 16; ++i) {
            const int key = (i & 3) + 8 * (i >> 2) + 4 * hh;
            sx[i] = key <= tok ? sx[i] : -1e30f;
            mx = fmaxf(mx, sx[i]);
        }
        mx = xmax32(mx);
        const float mc = mx * LOG2E;
        float l = 0.f;
#pragma unroll
        for (int i = 0; i < 16; ++i) { sx[i] = __builtin_amdgcn_exp2f(sx[i] * LOG2E - mc); l += sx[i]; }
#pragma unroll
        for (int i = 0; i < 4; ++i) { m4[i] = __builtin_amdgcn_exp2f(m4[i] * LOG2E - mc); l += m4[i]; }
        l = xsum32(l);
        const float inv = 1.0f / l;
#pragma unroll
        for (int k = 0; k < 2; ++k) {
            u32x4 v = {pk_bf(sx[8 * k] * inv, sx[8 * k + 1] * inv), pk_bf(sx[8 * k + 2] * inv, sx[8 * k + 3] * inv),
                       pk_bf(sx[8 * k + 4] * inv, sx[8 * k + 5] * inv), pk_bf(sx[8 * k + 6] * inv, sx[8 * k + 7] * inv)};
            *(u32x4*)(smem + LA_PX + ((w * 2 + k) * 64 + lane) * 16) = v;
        }
        *(u32x2*)(smem + LA_PM + (w * 64 + lane) * 8) = (u32x2){pk_bf(m4[0] * inv, m4[1] * inv), pk_bf(m4[2] * inv, m4[3] * inv)};
    }
    BAR();
#pragma unroll
    for (int t = 0; t < 2; ++t)
        if (t < nt) {
            f32x16 o;
#pragma unroll
            for (int i = 0; i < 16; ++i) o[i] = 0.f;
#pragma unroll
            for (int k = 0; k < 8; ++k) o = mfma32(vf[t][k], *(const u32x4*)(smem + LA_PX + (k * 64 + lane) * 16), o);
#pragma unroll
            for (int pp = 0; pp < 2; ++pp) {
                const u32x2 pa = *(const u32x2*)(smem + LA_PM + ((2 * pp) * 64 + lane) * 8), pb = *(const u32x2*)(smem + LA_PM + ((2 * pp + 1) * 64 + lane) * 8);
                o = mfma32((u32x4){vm[t][2 * pp].x, vm[t][2 * pp].y, vm[t][2 * pp + 1].x, vm[t][2 * pp + 1].y}, (u32x4){pa.x, pa.y, pb.x, pb.y}, o);
            }
#pragma unroll
            for (int gq = 0; gq < 4; ++gq) {
                f32x4 v = {o[4 * gq], o[4 * gq + 1], o[4 * gq + 2], o[4 * gq + 3]};
                ST16(__builtin_bit_cast(u32x4, v), rop, lane * 16 + gq * 1024, ((s * 4 + hg) * 14 + w + 8 * t) * 4096, SC1);
            }
        }
    asm volatile("s_waitcnt vmcnt(0)" ::: "memory");
    BAR();
    if (tid_opaque() == 0) *(volatile LAS unsigned*)(smem + LA_BC) = add_agent(ctrl + CT_A(s), 1u);
    BAR();
    const unsigned arrived = __builtin_amdgcn_readfirstlane(*(volatile LAS unsigned*)(smem + LA_BC));
    if (arrived == 3u) {
        float hv[2][16];
        float ssum = 0.f;
#pragma unroll
        for (int t = 0; t < 2; ++t)
            if (t < nt) {
#pragma unroll
                for (int gq = 0; gq < 4; ++gq) {
                    f32x4 a = __builtin_bit_cast(f32x4, LD16(rop, lane * 16 + gq * 1024, ((s * 4 + 0) * 14 + w + 8 * t) * 4096, SC1));
#pragma unroll
                    for (int k = 1; k < 4; ++k) a += __builtin_bit_cast(f32x4, LD16(rop, lane * 16 + gq * 1024, ((s * 4 + k) * 14 + w + 8 * t) * 4096, SC1));
                    const u32x2 xr = *(const u32x2*)(p.x + (size_t)(32 * s + tok) * DM + 32 * (w + 8 * t) + 8 * gq + 4 * hh);
                    const float xv[4] = {bf_lo(xr.x), bf_hi(xr.x), bf_lo(xr.y), bf_hi(xr.y)};
#pragma unroll
                    for (int e = 0; e < 4; ++e) { hv[t][4 * gq + e] = rbf(xv[e] + rbf(a[e])); ssum += hv[t][4 * gq + e]; }
                }
            }
        ssum = xsum32(ssum);
        float* red = (float*)(smem + LA_RED);
        if (lane < 32) red[w * 32 + tok] = ssum;
        BAR();
        float mean = 0.f;
#pragma unroll
        for (int k = 0; k < 8; ++k) mean += red[k * 32 + tok];
        mean *= (1.0f / DM);
        float s2 = 0.f;
#pragma unroll
        for (int t = 0; t < 2; ++t)
            if (t < nt) {
#pragma unroll
                for (int i = 0; i < 16; ++i) { const float d = hv[t][i] - mean; s2 += d * d; }
            }
        s2 = xsum32(s2);
        BAR();
        if (lane < 32) red[w * 32 + tok] = s2;
        BAR();
        float var = 0.f;
#pragma unroll
        for (int k = 0; k < 8; ++k) var += red[k * 32 + tok];
        var *= (1.0f / DM);
        const float mb = rbf(mean), den = rbf(sqrtf(rbf(rbf(var) + 1e-5f)));
#pragma unroll
        for (int t = 0; t < 2; ++t)
            if (t < nt) {
#pragma unroll
                for (int k = 0; k < 2; ++k) {
                    float y[8];
#pragma unroll
                    for (int e = 0; e < 8; ++e) y[e] = rbf(hv[t][8 * k + e] - mb) / den;
                    const size_t off = (size_t)s * SZ_SEGF + (size_t)(2 * (w + 8 * t) + k) * 1024 + lane * 16;
                    *(u32x4*)(p.ws + OFF_HD + off) = (u32x4){pk_bf(hv[t][8 * k], hv[t][8 * k + 1]), pk_bf(hv[t][8 * k + 2], hv[t][8 * k + 3]), pk_bf(hv[t][8 * k + 4], hv[t][8 * k + 5]), pk_bf(hv[t][8 * k + 6], hv[t][8 * k + 7])};
                    *(u32x4*)(p.ws + OFF_HNF + off) = (u32x4){pk_bf(y[0], y[1]), pk_bf(y[2], y[3]), pk_bf(y[4], y[5]), pk_bf(y[6], y[7])};
                }
            }
    }
    BAR();
}

constexpr int K2_LDS = GEMM_LDS + 256;

__global__ void __launch_bounds__(512, 2) k2_main(Params p) {
    extern __shared__ __attribute__((aligned(16))) char smem[];
    const int tid = threadIdx.x;
    unsigned* ctrl = (unsigned*)(p.ws + OFF_CTRL);
    volatile LAS unsigned* bc = (volatile LAS unsigned*)(smem + GEMM_LDS);
    if (tid == 0) {
        const unsigned xc = xcc_id();
        const unsigned r = add_agent(ctrl + CT_TICK(xc), 1u);
        unsigned role;
        if ((xc == 0u || xc == 2u) && r < 32u) role = (xc >> 1) * 32u + r;
        else role = N_REC + add_agent(ctrl + CT_WTICK, 1u);
        asm volatile("s_waitcnt vmcnt(0)" ::: "memory");
        add_agent(ctrl + CT_TOTAL, 1u);
        if (role >= N_WG) {
            unsigned sp = 0;
            while (ld_agent(ctrl + CT_TOTAL) < gridDim.x && ++sp < SPIN_CAP) __builtin_amdgcn_s_sleep(8);
            const unsigned c0 = min(ld_agent(ctrl + CT_TICK(0)), 32u), c2 = min(ld_agent(ctrl + CT_TICK(2)), 32u);
            const unsigned mi = role - N_WG;
            role = mi < 32u - c0 ? c0 + mi : 32u + c2 + (mi - (32u - c0));
        }
        bc[0] = role;
    }
    __syncthreads();
    const int role = __builtin_amdgcn_readfirstlane(bc[0]);
    __syncthreads();
    if (role < N_REC) { rec_role(p, smem, role); return; }
    if (role < N_REC + N_GEMM) { gemm448<0>(p, smem, role - N_REC, 0, 32); __syncthreads(); }
    for (;;) {
        if (tid_opaque() == 0) bc[0] = add_agent(ctrl + CT_ATICK, 1u);
        __syncthreads();
        const int item = __builtin_amdgcn_readfirstlane(bc[0]);
        __syncthreads();
        if (item >= 4 * NSEG) break;
        if (tid_opaque() < 64) STAMP(256 + item, 0);
        attn_item(p, smem, item >> 2, item & 3);
        if (tid_opaque() < 64) STAMP(256 + item, 1);
    }
}

__global__ void __launch_bounds__(512, 2) k3_ffn1(Params p) {
    extern __shared__ __attribute__((aligned(16))) char smem[];
    gemm448<1>(p, smem, blockIdx.x % 14, (blockIdx.x / 14) * 4, 2);
}

constexpr int K4_LDS = 2 * 65536;
__global__ void __launch_bounds__(512, 2) k4_ffn2(Params p) {
    extern __shared__ __attribute__((aligned(16))) char smem[];
    const int tid = threadIdx.x, lane = tid & 63, w = __builtin_amdgcn_readfirstlane(tid >> 6);
    const int cb = blockIdx.x % 7, sb = blockIdx.x / 7;
    const int ct = w & 1, sg = (w >> 1) & 1, kh = w >> 2;
    const char* w2f = p.ws + OFF_W2F + (size_t)(2 * cb) * 112 * 1024;
    const char* rf = p.ws + OFF_RF + (size_t)(2 * sb) * 112 * 1024;
    auto stage = [&](int it, int slot) {
#pragma unroll
        for (int i = 0; i < 8; ++i) {
            const int pc = w * 8 + i, pk_h = pc >> 5, pks = (pc >> 2) & 7, pw = pc & 3;
            const int ks = pk_h * 56 + it * 8 + pks;
            const char* sp = (pw < 2 ? w2f + (size_t)pw * 112 * 1024 : rf + (size_t)(pw - 2) * 112 * 1024) + (size_t)ks * 1024 + lane * 16;
            GLDS(sp, smem + slot * 65536 + pc * 1024, 0);
        }
    };
    stage(0, 0); stage(1, 1);
    f32x16 acc;
#pragma unroll
    for (int i = 0; i < 16; ++i) acc[i] = 0.f;
    for (int it = 0; it < 7; ++it) {
        if (it < 6) asm volatile("s_waitcnt vmcnt(8)" ::: "memory"); else asm volatile("s_waitcnt vmcnt(0)" ::: "memory");
        BAR();
        const char* base = smem + (it & 1) * 65536 + kh * 32768 + lane * 16;
#pragma unroll
        for (int k = 0; k < 8; ++k) acc = mfma32(*(const u32x4*)(base + (k * 4 + ct) * 1024), *(const u32x4*)(base + (k * 4 + 2 + sg) * 1024), acc);
        BAR();
        if (it + 2 < 7) stage(it + 2, it & 1);
    }
    if (kh == 1) {
#pragma unroll
        for (int gq = 0; gq < 4; ++gq) *(f32x4*)(smem + ((w & 3) * 4 + gq) * 1024 + lane * 16) = (f32x4){acc[4 * gq], acc[4 * gq + 1], acc[4 * gq + 2], acc[4 * gq + 3]};
    }
    BAR();
    if (kh == 0) {
        const int tok = lane & 31, hh = lane >> 5, s = 2 * sb + sg, ctile = 2 * cb + ct;
        unsigned pk[8];
#pragma unroll
        for (int k = 0; k < 2; ++k) {
            const u32x4 hr = *(const u32x4*)(p.ws + OFF_HD + (size_t)s * SZ_SEGF + (size_t)(2 * ctile + k) * 1024 + lane * 16);
            const float hvv[8] = {bf_lo(hr.x), bf_hi(hr.x), bf_lo(hr.y), bf_hi(hr.y), bf_lo(hr.z), bf_hi(hr.z), bf_lo(hr.w), bf_hi(hr.w)};
#pragma unroll
            for (int gg = 0; gg < 2; ++gg) {
                const int gq = 2 * k + gg;
                const f32x4 o2 = *(const f32x4*)(smem + ((w & 3) * 4 + gq) * 1024 + lane * 16);
                const u32x2 br = *(const u32x2*)(p.b2 + 32 * ctile + 8 * gq + 4 * hh);
                const float bv[4] = {bf_lo(br.x), bf_hi(br.x), bf_lo(br.y), bf_hi(br.y)};
                float y[4];
#pragma unroll
                for (int e = 0; e < 4; ++e) y[e] = hvv[4 * gg + e] + rbf(rbf(acc[4 * gq + e] + o2[e]) + bv[e]);
                pk[2 * gq] = pk_bf(y[0], y[1]); pk[2 * gq + 1] = pk_bf(y[2], y[3]);
            }
        }
#pragma unroll
        for (int k = 0; k < 2; ++k) {
            auto r0 = __builtin_amdgcn_permlane32_swap(pk[4 * k], pk[4 * k + 2], false, false);
            auto r1 = __builtin_amdgcn_permlane32_swap(pk[4 * k + 1], pk[4 * k + 3], false, false);
            *(u32x4*)(p.out + (size_t)(32 * s + tok) * DM + 32 * ctile + 16 * k + 8 * hh) = (u32x4){r0[0], r1[0], r0[1], r1[1]};
        }
    }
}

extern "C" void kernel_launch(void* const* d_in, const int* in_sizes, int n_in, void* d_out, int out_size, void* d_ws, size_t ws_size, hipStream_t stream) {
    Params p;
    p.x = (const uint16_t*)d_in[0]; p.mem0 = (const uint16_t*)d_in[1];
    p.wq = (const uint16_t*)d_in[2]; p.wk = (const uint16_t*)d_in[3]; p.wv = (const uint16_t*)d_in[4]; p.wo = (const uint16_t*)d_in[5];
    p.w1 = (const uint16_t*)d_in[8]; p.b1 = (const uint16_t*)d_in[9]; p.w2 = (const uint16_t*)d_in[10]; p.b2 = (const uint16_t*)d_in[11];
    p.out = (uint16_t*)d_out; p.ws = (char*)d_ws;
    uintptr_t hx = ((uintptr_t)d_ws + OFF_END + ((size_t)2 << 20) - 1) & ~(uintptr_t)(((size_t)2 << 20) - 1);
    p.hx = (char*)hx;
    (void)hipFuncSetAttribute((const void*)k1_prep, hipFuncAttributeMaxDynamicSharedMemorySize, K1_LDS);
    (void)hipFuncSetAttribute((const void*)k2_main, hipFuncAttributeMaxDynamicSharedMemorySize, K2_LDS);
    (void)hipFuncSetAttribute((const void*)k3_ffn1, hipFuncAttributeMaxDynamicSharedMemorySize, GEMM_LDS);
    (void)hipFuncSetAttribute((const void*)k4_ffn2, hipFuncAttributeMaxDynamicSharedMemorySize, K4_LDS);
    hipLaunchKernelGGL(k1_prep, dim3(K1_GRID), dim3(256), K1_LDS, stream, p);
    hipLaunchKernelGGL(k2_main, dim3(N_WG), dim3(512), K2_LDS, stream, p);
    hipLaunchKernelGGL(k3_ffn1, dim3(224), dim3(512), GEMM_LDS, stream, p);
    hipLaunchKernelGGL(k4_ffn2, dim3(224), dim3(512), K4_LDS, stream, p);
}
```

```cpp
#include <hip/hip_runtime.h>
#include <hip/hip_bf16.h>
#include <stdint.h>

typedef __bf16 bf16x8 __attribute__((ext_vector_type(8)));
typedef __bf16 bf16x2 __attribute__((ext_vector_type(2)));
typedef short s16x4 __attribute__((ext_vector_type(4)));
typedef float f32x2 __attribute__((ext_vector_type(2)));
typedef float f32x4 __attribute__((ext_vector_type(4)));
typedef float f32x16 __attribute__((ext_vector_type(16)));
typedef unsigned u32x2 __attribute__((ext_vector_type(2)));
typedef unsigned u32x4 __attribute__((ext_vector_type(4)));
#define LAS __attribute__((address_space(3)))
#define GAS __attribute__((address_space(1)))
#define DI __device__ __forceinline__
typedef __amdgpu_buffer_rsrc_t rsrc_t;

constexpr int DM = 448, NH = 16, DH = 256, SEQ = 2048, NSEG = 64, FF = 1792;

constexpr size_t OFF_CTRL = 0;
constexpr size_t SZ_CTRL = 262144;
constexpr size_t OFF_XNF = OFF_CTRL + SZ_CTRL;
constexpr size_t SZ_SEGF = 28 * 1024;
constexpr size_t OFF_WCT = OFF_XNF + 64 * SZ_SEGF;
constexpr size_t SZ_WCT = (size_t)448 * 56 * 512;
constexpr size_t OFF_KT = OFF_WCT + SZ_WCT;
constexpr size_t SZ_KT = (size_t)64 * 16 * 28672;
constexpr size_t OFF_VT = OFF_KT + SZ_KT;
constexpr size_t OFF_KM = OFF_VT + SZ_KT;
constexpr size_t SZ_KM = (size_t)64 * 16 * 7168;
constexpr size_t OFF_VM = OFF_KM + SZ_KM;
constexpr size_t OFF_OP = OFF_VM + SZ_KM;
constexpr size_t SZ_OP = (size_t)64 * 4 * 57344;
constexpr size_t OFF_HD = OFF_OP + SZ_OP;
constexpr size_t OFF_HNF = OFF_HD + 64 * SZ_SEGF;
constexpr size_t OFF_W1F = OFF_HNF + 64 * SZ_SEGF;
constexpr size_t SZ_W1F = (size_t)56 * 28 * 1024;
constexpr size_t OFF_W2F = OFF_W1F + SZ_W1F;
constexpr size_t OFF_RF = OFF_W2F + SZ_W1F;
constexpr size_t SZ_RF = (size_t)64 * 112 * 1024;
constexpr size_t OFF_END = OFF_RF + SZ_RF;
constexpr size_t SZ_HX = (size_t)4 << 20;

#define CT_TICK(x) (64 * (x))
#define CT_WTICK (64 * 16)
#define CT_TOTAL (64 * 17)
#define CT_ATICK (64 * 18)
#define CT_ABORT (64 * 19)
#define CT_PROBE(c) (57344 + (c) * 32)
#define CT_START(sh) (64 * (22 + (sh)))
#define CT_A(s) (64 * (32 + (s)))
#define CT_H(s) (49152 + (s) * 32)
#define CT_R(s) (49152 + 2048 + (s) * 32)
#define CT_KV(s, sh) (8192 + ((s) * 8 + (sh)) * 32)
#define CT_M(s, sh) (8192 + 16384 + ((s) * 8 + (sh)) * 32)
constexpr unsigned KV_PER_SHARD = 14, M_PER_SHARD = 8;

#define STAMP(wg, idx) do {} while (0)
#define RSTAMP(wv, idx) do {} while (0)
#define RVAL(wv, idx, val) do {} while (0)
constexpr int N_REC = 64, N_GEMM = 112, N_F1 = 28, N_F2 = 28, N_WG = 256;
constexpr unsigned SPIN_CAP = 1u << 15;
constexpr float LOG2E = 1.4426950408889634f;

struct Params {
    const uint16_t* x; const uint16_t* mem0; const uint16_t* wq; const uint16_t* wk; const uint16_t* wv; const uint16_t* wo;
    const uint16_t* w1; const uint16_t* b1; const uint16_t* w2; const uint16_t* b2;
    uint16_t* out; char* ws; char* hx;
};

DI float bf_lo(unsigned v) { return __builtin_bit_cast(float, v << 16); }
DI float bf_hi(unsigned v) { return __builtin_bit_cast(float, v & 0xffff0000u); }
DI unsigned pk_bf(float a, float b) { bf16x2 r = {(__bf16)a, (__bf16)b}; return __builtin_bit_cast(unsigned, r); }
DI float rbf(float a) { return bf_lo(pk_bf(a, a)); }
DI float dpp_ror8(float v) { return __builtin_bit_cast(float, __builtin_amdgcn_update_dpp(0, __builtin_bit_cast(int, v), 0x128, 0xf, 0xf, true)); }
DI unsigned fb(float v) { return __builtin_bit_cast(unsigned, v); }
DI float bfl(unsigned v) { return __builtin_bit_cast(float, v); }
DI float xsum32(float v) { auto r = __builtin_amdgcn_permlane32_swap(fb(v), fb(v), false, false); return bfl(r[0]) + bfl(r[1]); }
DI float xsum16(float v) { auto r = __builtin_amdgcn_permlane16_swap(fb(v), fb(v), false, false); return bfl(r[0]) + bfl(r[1]); }
DI float xmax32(float v) { auto r = __builtin_amdgcn_permlane32_swap(fb(v), fb(v), false, false); return fmaxf(bfl(r[0]), bfl(r[1])); }
DI float xmax16(float v) { auto r = __builtin_amdgcn_permlane16_swap(fb(v), fb(v), false, false); return fmaxf(bfl(r[0]), bfl(r[1])); }
DI f32x4 mfma16(u32x4 a, u32x4 b, f32x4 c) { return __builtin_amdgcn_mfma_f32_16x16x32_bf16(__builtin_bit_cast(bf16x8, a), __builtin_bit_cast(bf16x8, b), c, 0, 0, 0); }
DI f32x4 mfma16k16(u32x2 a, u32x2 b, f32x4 c) { return __builtin_amdgcn_mfma_f32_16x16x16bf16_1k(__builtin_bit_cast(s16x4, a), __builtin_bit_cast(s16x4, b), c, 0, 0, 0); }
DI f32x16 mfma32(u32x4 a, u32x4 b, f32x16 c) { return __builtin_amdgcn_mfma_f32_32x32x16_bf16(__builtin_bit_cast(bf16x8, a), __builtin_bit_cast(bf16x8, b), c, 0, 0, 0); }
DI rsrc_t mk_rsrc(const void* p, unsigned bytes) { return __builtin_amdgcn_make_buffer_rsrc((void*)p, 0, bytes, 0x00020000); }
#define LD16(r, vo, so, aux) __builtin_amdgcn_raw_buffer_load_b128((r), (vo), (so), (aux))
#define LD8(r, vo, so, aux) __builtin_amdgcn_raw_buffer_load_b64((r), (vo), (so), (aux))
#define ST16(v, r, vo, so, aux) do { const u32x4 st16_v_ = (v); __builtin_amdgcn_raw_buffer_store_b128(st16_v_, (r), (vo), (so), (aux)); asm volatile("s_nop 1" :: "v"(st16_v_)); } while (0)
#define ST8(v, r, vo, so, aux) __builtin_amdgcn_raw_buffer_store_b64((v), (r), (vo), (so), (aux))
#define ST4(v, r, vo, so, aux) __builtin_amdgcn_raw_buffer_store_b32((v), (r), (vo), (so), (aux))
constexpr int SC1 = 16;
#define BAR() do { asm volatile("s_waitcnt lgkmcnt(0)" ::: "memory"); __builtin_amdgcn_s_barrier(); asm volatile("" ::: "memory"); } while (0)
#define GLDS(src, dst, aux) __builtin_amdgcn_global_load_lds((const GAS void*)(src), (LAS void*)(dst), 16, 0, (aux))
DI unsigned ld_agent(const unsigned* p) { return __hip_atomic_load((const GAS unsigned*)p, __ATOMIC_RELAXED, __HIP_MEMORY_SCOPE_AGENT); }
DI void st_agent(unsigned* p, unsigned v) { __hip_atomic_store((GAS unsigned*)p, v, __ATOMIC_RELAXED, __HIP_MEMORY_SCOPE_AGENT); }
DI unsigned add_agent(unsigned* p, unsigned v) { return __hip_atomic_fetch_add((GAS unsigned*)p, v, __ATOMIC_RELAXED, __HIP_MEMORY_SCOPE_AGENT); }
DI int tid_opaque() { int t = threadIdx.x; asm volatile("" : "+v"(t)); return t; }
DI unsigned xcc_id() { return (unsigned)__builtin_amdgcn_s_getreg(20 | (3 << 11)) & 0xfu; }
DI float wave_sum(float v) {
    v += dpp_ror8(v);
    v += __builtin_bit_cast(float, __builtin_amdgcn_update_dpp(0, __builtin_bit_cast(int, v), 0x124, 0xf, 0xf, true));
    v += __builtin_bit_cast(float, __builtin_amdgcn_update_dpp(0, __builtin_bit_cast(int, v), 0x122, 0xf, 0xf, true));
    v += __builtin_bit_cast(float, __builtin_amdgcn_update_dpp(0, __builtin_bit_cast(int, v), 0x121, 0xf, 0xf, true));
    return xsum32(xsum16(v));
}
DI void wait_count(unsigned* ctrl, int word, unsigned target, int slp) {
    for (unsigned sp = 0; sp < SPIN_CAP; ++sp) {
        unsigned v = __builtin_amdgcn_readfirstlane(ld_agent(ctrl + word));
        if (v >= target) return;
        __builtin_amdgcn_s_sleep(2);
        for (int i = 0; i < slp; ++i) __builtin_amdgcn_s_sleep(8);
        if ((sp & 255u) == 255u && __builtin_amdgcn_readfirstlane(ld_agent(ctrl + CT_ABORT))) return;
    }
    st_agent(ctrl + CT_ABORT, 1u);
}

DI void wait_shards(unsigned* ctrl, int word, unsigned target, int slp) {
    const int lane = threadIdx.x & 7;
    for (unsigned sp = 0; sp < SPIN_CAP; ++sp) {
        if (!__any(ld_agent(ctrl + word + lane * 32) < target)) return;
        __builtin_amdgcn_s_sleep(2);
        for (int i = 0; i < slp; ++i) __builtin_amdgcn_s_sleep(8);
        if ((sp & 63u) == 63u && __builtin_amdgcn_readfirstlane(ld_agent(ctrl + CT_ABORT))) return;
    }
    st_agent(ctrl + CT_ABORT, 1u);
}

constexpr int K1_AB = 512, K1_LN = 128, K1_W1 = 56, K1_W2 = 56;
constexpr int K1_GRID = K1_AB + K1_LN + K1_W1 + K1_W2 + 1;
constexpr int K1_LDS = 2 * 112 * 272 + 1024;

__global__ void __launch_bounds__(256, 2) k1_prep(Params p) {
    extern __shared__ __attribute__((aligned(16))) char smem[];
    const int b = blockIdx.x, tid = threadIdx.x, lane = tid & 63, w = tid >> 6;
    if (b < K1_AB) {
        const int m = (b & 7) + 8 * (b >> 7), tr = (b >> 5) & 3, tc = (b >> 3) & 3, hd = m & 15;
        const bool isA = m < 16;
        const uint16_t* arow = (isA ? p.wk : p.wv) + (size_t)(112 * tr) * 4096 + hd * 256;
        const uint16_t* brow = p.wq + (size_t)(112 * tc) * 4096 + hd * 256;
        const uint16_t* bcol = p.wo + (size_t)hd * 256 * 448 + 112 * tc;
        char* As = smem; char* Bs = smem + 112 * 272;
        u32x4 ra[2][7], rb[2][7];
#pragma unroll
        for (int kh = 0; kh < 2; ++kh)
#pragma unroll
            for (int i = 0; i < 7; ++i) {
                const int ch = tid + 256 * i;
                ra[kh][i] = *(const u32x4*)(arow + (size_t)(ch >> 4) * 4096 + kh * 128 + (ch & 15) * 8);
                if (isA) rb[kh][i] = *(const u32x4*)(brow + (size_t)(ch >> 4) * 4096 + kh * 128 + (ch & 15) * 8);
                else     rb[kh][i] = *(const u32x4*)(bcol + (size_t)(kh * 128 + ch / 14) * 448 + (ch % 14) * 8);
            }
        f32x4 acc[2][7];
#pragma unroll
        for (int i = 0; i < 2; ++i)
#pragma unroll
            for (int j = 0; j < 7; ++j) acc[i][j] = (f32x4){0.f, 0.f, 0.f, 0.f};
        const int x = lane & 15, q = lane >> 4;
#pragma unroll
        for (int kh = 0; kh < 2; ++kh) {
            if (kh) __syncthreads();
#pragma unroll
            for (int i = 0; i < 7; ++i) {
                const int ch = tid + 256 * i;
                *(u32x4*)(As + (ch >> 4) * 272 + (ch & 15) * 16) = ra[kh][i];
                if (isA) *(u32x4*)(Bs + (ch >> 4) * 272 + (ch & 15) * 16) = rb[kh][i];
                else     *(u32x4*)(Bs + (ch / 14) * 240 + (ch % 14) * 16) = rb[kh][i];
            }
            __syncthreads();
#pragma unroll
            for (int ks = 0; ks < 4; ++ks) {
                u32x4 bf[7];
#pragma unroll
                for (int j = 0; j < 7; ++j) {
                    if (isA) bf[j] = *(const u32x4*)(Bs + (16 * j + x) * 272 + ks * 64 + q * 16);
                    else {
                        const int r0 = 32 * ks + 8 * q + (x >> 2), cb = (16 * j + 4 * (x & 3)) * 2;
                        s16x4 lo = __builtin_amdgcn_ds_read_tr16_b64_v4i16((LAS s16x4*)(Bs + r0 * 240 + cb));
                        s16x4 hi = __builtin_amdgcn_ds_read_tr16_b64_v4i16((LAS s16x4*)(Bs + (r0 + 4) * 240 + cb));
                        u32x2 l2 = __builtin_bit_cast(u32x2, lo), h2 = __builtin_bit_cast(u32x2, hi);
                        bf[j] = (u32x4){l2.x, l2.y, h2.x, h2.y};
                    }
                }
#pragma unroll
                for (int i = 0; i < 2; ++i) {
                    const int rt = w + 4 * i;
                    if (rt < 7) {
                        const u32x4 af = *(const u32x4*)(As + (16 * rt + x) * 272 + ks * 64 + q * 16);
#pragma unroll
                        for (int j = 0; j < 7; ++j) acc[i][j] = mfma16(af, bf[j], acc[i][j]);
                    }
                }
            }
        }
        const float sc = isA ? 0.0625f : 1.0f;
        char* wct = p.ws + OFF_WCT;
#pragma unroll
        for (int i = 0; i < 2; ++i) {
            const int rt = w + 4 * i;
            if (rt < 7) {
#pragma unroll
                for (int j = 0; j < 7; ++j) {
                    const int np = (isA ? 0 : 7168) + hd * 448 + 112 * tc + 16 * j + x;
                    const int k = 112 * tr + 16 * rt + 4 * q;
                    u32x2 v = {pk_bf(acc[i][j][0] * sc, acc[i][j][1] * sc), pk_bf(acc[i][j][2] * sc, acc[i][j][3] * sc)};
                    *(u32x2*)(wct + ((size_t)((np >> 5) * 56 + (k >> 3)) * 32 + (np & 31)) * 16 + ((k >> 2) & 1) * 8) = v;
                }
            }
        }
        return;
    }
    int bb = b - K1_AB;
    if (bb < K1_LN) {
#pragma unroll
        for (int i = 0; i < 4; ++i) {
            const int row = bb * 16 + w * 4 + i;
            float v[8];
            u32x4 raw = {0u, 0u, 0u, 0u};
            if (lane < 56) raw = *(const u32x4*)(p.x + (size_t)row * DM + lane * 8);
            v[0] = bf_lo(raw.x); v[1] = bf_hi(raw.x); v[2] = bf_lo(raw.y); v[3] = bf_hi(raw.y);
            v[4] = bf_lo(raw.z); v[5] = bf_hi(raw.z); v[6] = bf_lo(raw.w); v[7] = bf_hi(raw.w);
            float s = 0.f;
#pragma unroll
            for (int e = 0; e < 8; ++e) s += v[e];
            const float mean = wave_sum(s) * (1.0f / DM);
            float s2 = 0.f;
            if (lane < 56) {
#pragma unroll
                for (int e = 0; e < 8; ++e) { const float d = v[e] - mean; s2 += d * d; }
            }
            const float var = wave_sum(s2) * (1.0f / DM);
            const float mb = rbf(mean);
            const float den = rbf(sqrtf(rbf(rbf(var) + 1e-5f)));
            if (lane < 56) {
                float y[8];
#pragma unroll
                for (int e = 0; e < 8; ++e) y[e] = rbf(v[e] - mb) / den;
                u32x4 o = {pk_bf(y[0], y[1]), pk_bf(y[2], y[3]), pk_bf(y[4], y[5]), pk_bf(y[6], y[7])};
                *(u32x4*)(p.ws + OFF_XNF + (size_t)(row >> 5) * SZ_SEGF + ((lane >> 1) * 64 + (lane & 1) * 32 + (row & 31)) * 16) = o;
            }
        }
        return;
    }
    bb -= K1_LN;
    if (bb < K1_W1 + K1_W2) {
        const bool is1 = bb < K1_W1;
        const uint16_t* src; int stride; char* dst;
        if (is1) { src = p.w1 + 32 * bb; stride = FF; dst = p.ws + OFF_W1F + (size_t)bb * 28 * 1024; }
        else { const int t = bb - K1_W1, ct = t >> 2, fq = t & 3; src = p.w2 + (size_t)(448 * fq) * DM + 32 * ct; stride = DM; dst = p.ws + OFF_W2F + ((size_t)ct * 112 + 28 * fq) * 1024; }
#pragma unroll
        for (int i = 0; i < 7; ++i) {
            const int ch = tid + 256 * i;
            *(u32x4*)(smem + (ch >> 2) * 80 + (ch & 3) * 16) = *(const u32x4*)(src + (size_t)(ch >> 2) * stride + (ch & 3) * 8);
        }
        __syncthreads();
        const int hh = lane >> 5, blk = (lane >> 4) & 1, a = (lane & 15) >> 2, pp = lane & 3;
#pragma unroll
        for (int i = 0; i < 7; ++i) {
            const int ks = w + 4 * i;
            const int r0 = 16 * ks + 4 * hh + a, cb = (16 * blk + 4 * pp) * 2;
            s16x4 lo = __builtin_amdgcn_ds_read_tr16_b64_v4i16((LAS s16x4*)(smem + r0 * 80 + cb));
            s16x4 hi = __builtin_amdgcn_ds_read_tr16_b64_v4i16((LAS s16x4*)(smem + (r0 + 8) * 80 + cb));
            u32x2 l2 = __builtin_bit_cast(u32x2, lo), h2 = __builtin_bit_cast(u32x2, hi);
            *(u32x4*)(dst + (size_t)ks * 1024 + lane * 16) = (u32x4){l2.x, l2.y, h2.x, h2.y};
        }
        return;
    }
    {
        const u32x4 z = {0u, 0u, 0u, 0u};
#pragma unroll
        for (int i = 0; i < 64; ++i) *(u32x4*)(p.ws + OFF_CTRL + ((size_t)i * 256 + tid) * 16) = z;
    }
}

constexpr int GEMM_LDS = 2 * 2 * 28 * 1024;

template <int MODE>
DI void gemm448(const Params& p, char* smem, int slice, int seg0, int npair) {
    const int tid = threadIdx.x, lane = tid & 63, w = __builtin_amdgcn_readfirstlane(tid >> 6);
    const int ct = w & 3, th = w >> 2, T = 4 * slice + ct;
    unsigned* ctrl = (unsigned*)(p.ws + OFF_CTRL);
    const char* src = p.ws + (MODE == 0 ? OFF_XNF : OFF_HNF) + (size_t)seg0 * SZ_SEGF;
    u32x4 wf[28];
    if (MODE == 0) {
        const char* wb = p.ws + OFF_WCT + (size_t)T * 28672 + lane * 16;
#pragma unroll
        for (int ks = 0; ks < 28; ++ks) wf[ks] = *(const u32x4*)(wb + ks * 1024);
    } else {
        const char* wb = p.ws + OFF_W1F + (size_t)T * 28672 + lane * 16;
#pragma unroll
        for (int ks = 0; ks < 28; ++ks) wf[ks] = *(const u32x4*)(wb + ks * 1024);
    }
#pragma unroll
    for (int pr = 0; pr < 2; ++pr)
        if (pr < npair) {
#pragma unroll
            for (int i = 0; i < 7; ++i) GLDS(src + (size_t)pr * 57344 + (w * 7 + i) * 1024 + lane * 16, smem + pr * 57344 + (w * 7 + i) * 1024, 0);
        }
    asm volatile("s_waitcnt vmcnt(0)" ::: "memory");
    if (MODE == 0 && w == 0) STAMP(64 + slice, 1);
    const bool isK = (MODE == 0) && (T < 224);
    const int Th = isK ? T : T - 224, hd = Th / 14, ctile = Th % 14;
    const rsrc_t ro = (MODE == 0) ? mk_rsrc(p.ws + (isK ? OFF_KT : OFF_VT), (unsigned)SZ_KT) : mk_rsrc(p.ws + OFF_RF, (unsigned)SZ_RF);
    u32x2 bias[4];
    if (MODE == 1) {
#pragma unroll
        for (int g = 0; g < 4; ++g) bias[g] = *(const u32x2*)(p.b1 + 32 * T + 8 * g + 4 * (lane >> 5));
    }
    for (int pr = 0; pr < npair; ++pr) {
        if (pr >= 1) {
            if (pr + 1 < npair) asm volatile("s_waitcnt vmcnt(7)" ::: "memory"); else asm volatile("s_waitcnt vmcnt(0)" ::: "memory");
        }
        BAR();
        if (MODE == 0 && w == 0) STAMP(64 + slice, 2 + pr);
        if (MODE == 0 && pr >= 1 && tid_opaque() < 2) add_agent(ctrl + CT_KV(seg0 + 2 * (pr - 1) + tid, slice & 7), 1u);
        const char* xs = smem + (pr & 1) * 57344 + th * 28672 + lane * 16;
        f32x16 acc;
#pragma unroll
        for (int i = 0; i < 16; ++i) acc[i] = 0.f;
        if (MODE == 0 && !isK) {
#pragma unroll
            for (int ks = 0; ks < 28; ++ks) acc = mfma32(*(const u32x4*)(xs + ks * 1024), wf[ks], acc);
        } else {
#pragma unroll
            for (int ks = 0; ks < 28; ++ks) acc = mfma32(wf[ks], *(const u32x4*)(xs + ks * 1024), acc);
        }
        const int s = seg0 + 2 * pr + th;
        if (MODE == 0) {
            unsigned pk[8];
#pragma unroll
            for (int i = 0; i < 8; ++i) pk[i] = pk_bf(acc[2 * i], acc[2 * i + 1]);
            if (isK) {
#pragma unroll
                for (int k = 0; k < 2; ++k) {
                    auto r0 = __builtin_amdgcn_permlane32_swap(pk[4 * k], pk[4 * k + 2], false, false);
                    auto r1 = __builtin_amdgcn_permlane32_swap(pk[4 * k + 1], pk[4 * k + 3], false, false);
                    u32x4 v = {r0[0], r1[0], r0[1], r1[1]};
                    ST16(v, ro, lane * 16 + k * 1024, ((s * 16 + hd) * 56 + 4 * ctile) * 512, SC1);
                }
            } else {
#pragma unroll
                for (int k = 0; k < 2; ++k) {
                    u32x4 v = {pk[4 * k], pk[4 * k + 1], pk[4 * k + 2], pk[4 * k + 3]};
                    ST16(v, ro, lane * 16 + k * 1024, ((s * 16 + hd) * 14 + ctile) * 2048, SC1);
                }
            }
        } else {
            unsigned pk[8];
#pragma unroll
            for (int g = 0; g < 4; ++g) {
                float bv[4] = {bf_lo(bias[g].x), bf_hi(bias[g].x), bf_lo(bias[g].y), bf_hi(bias[g].y)};
                float r[4];
#pragma unroll
                for (int e = 0; e < 4; ++e) { float t = rbf(rbf(acc[4 * g + e]) + bv[e]); t = t > 0.f ? t : 0.f; r[e] = t * t; }
                pk[2 * g] = pk_bf(r[0], r[1]); pk[2 * g + 1] = pk_bf(r[2], r[3]);
            }
#pragma unroll
            for (int k = 0; k < 2; ++k) {
                u32x4 v = {pk[4 * k], pk[4 * k + 1], pk[4 * k + 2], pk[4 * k + 3]};
                ST16(v, ro, lane * 16 + k * 1024, (s * 112 + 2 * T) * 1024, 0);
            }
        }
        if (pr + 2 < npair) {
            BAR();
#pragma unroll
            for (int i = 0; i < 7; ++i) GLDS(src + (size_t)(pr + 2) * 57344 + (w * 7 + i) * 1024 + lane * 16, smem + (pr & 1) * 57344 + (w * 7 + i) * 1024, 0);
        }
    }
    if (MODE == 0) {
        asm volatile("s_waitcnt vmcnt(0)" ::: "memory");
        BAR();
        if (tid_opaque() < 2) add_agent(ctrl + CT_KV(seg0 + 2 * (npair - 1) + tid, slice & 7), 1u);
    }
}

DI void ffn1_role(const Params& p, char* smem, int slice, int par) {
    const int tid = threadIdx.x, lane = tid & 63, w = __builtin_amdgcn_readfirstlane(tid >> 6);
    const int ct = w & 3, kh = w >> 2, T = 4 * slice + ct, hh = lane >> 5;
    unsigned* ctrl = (unsigned*)(p.ws + OFF_CTRL);
    u32x4 wf[14];
    {
        const char* wb = p.ws + OFF_W1F + (size_t)T * 28672 + (size_t)kh * 14336 + lane * 16;
#pragma unroll
        for (int ks = 0; ks < 14; ++ks) wf[ks] = *(const u32x4*)(wb + ks * 1024);
    }
    u32x2 bias[4];
#pragma unroll
    for (int g = 0; g < 4; ++g) bias[g] = *(const u32x2*)(p.b1 + 32 * T + 8 * g + 4 * hh);
    const rsrc_t ro = mk_rsrc(p.ws + OFF_RF, (unsigned)SZ_RF);
    char* red = smem + 28672;
    for (int s = par; s < NSEG; s += 2) {
        if (w == 0) wait_count(ctrl, CT_H(s), 1u, s >= 60 ? 0 : 4);
        BAR();
        {
            const char* src = p.ws + OFF_HNF + (size_t)s * SZ_SEGF;
#pragma unroll
            for (int i = 0; i < 4; ++i) { const int k = w + 8 * i; if (k < 28) GLDS(src + k * 1024 + lane * 16, smem + k * 1024, SC1); }
        }
        asm volatile("s_waitcnt vmcnt(0)" ::: "memory");
        BAR();
        f32x16 acc;
#pragma unroll
        for (int i = 0; i < 16; ++i) acc[i] = 0.f;
        u32x4 xf[14];
#pragma unroll
        for (int ks = 0; ks < 14; ++ks) xf[ks] = *(const u32x4*)(smem + (14 * kh + ks) * 1024 + lane * 16);
        __builtin_amdgcn_sched_barrier(0);
#pragma unroll
        for (int ks = 0; ks < 14; ++ks) acc = mfma32(wf[ks], xf[ks], acc);
        if (kh == 1) {
#pragma unroll
            for (int g = 0; g < 4; ++g) *(f32x4*)(red + ((ct * 4 + g) * 64 + lane) * 16) = (f32x4){acc[4 * g], acc[4 * g + 1], acc[4 * g + 2], acc[4 * g + 3]};
        }
        BAR();
        if (kh == 0) {
            unsigned pk[8];
#pragma unroll
            for (int g = 0; g < 4; ++g) {
                const f32x4 o2 = *(const f32x4*)(red + ((ct * 4 + g) * 64 + lane) * 16);
                const float bv[4] = {bf_lo(bias[g].x), bf_hi(bias[g].x), bf_lo(bias[g].y), bf_hi(bias[g].y)};
                float r[4];
#pragma unroll
                for (int e = 0; e < 4; ++e) { float t = rbf(rbf(acc[4 * g + e] + o2[e]) + bv[e]); t = t > 0.f ? t : 0.f; r[e] = t * t; }
                pk[2 * g] = pk_bf(r[0], r[1]); pk[2 * g + 1] = pk_bf(r[2], r[3]);
            }
#pragma unroll
            for (int k = 0; k < 2; ++k) ST16(((u32x4){pk[4 * k], pk[4 * k + 1], pk[4 * k + 2], pk[4 * k + 3]}), ro, lane * 16 + k * 1024, (s * 112 + 2 * T) * 1024, SC1);
            asm volatile("s_waitcnt vmcnt(0)" ::: "memory");
        }
        BAR();
        if (tid_opaque() == 0) add_agent(ctrl + CT_R(s), 1u);
    }
}
DI void ffn2_role(const Params& p, char* smem, int ctile, int par) {
    const int tid = threadIdx.x, lane = tid & 63, w = __builtin_amdgcn_readfirstlane(tid >> 6);
    const int tok = lane & 31, hh = lane >> 5;
    unsigned* ctrl = (unsigned*)(p.ws + OFF_CTRL);
    u32x4 wf[14];
    {
        const char* wb = p.ws + OFF_W2F + ((size_t)ctile * 112 + 14 * w) * 1024 + lane * 16;
#pragma unroll
        for (int ks = 0; ks < 14; ++ks) wf[ks] = *(const u32x4*)(wb + ks * 1024);
    }
    const u32x2 br = *(const u32x2*)(p.b2 + 32 * ctile + 8 * (w & 3) + 4 * hh);
    const rsrc_t rr = mk_rsrc(p.ws + OFF_RF, (unsigned)SZ_RF), rh = mk_rsrc(p.ws + OFF_HD, (unsigned)(64 * SZ_SEGF));
    for (int s = par; s < NSEG; s += 2) {
        if (w == 0) wait_count(ctrl, CT_R(s), 14u, s >= 60 ? 0 : 4);
        BAR();
        u32x4 xf[14];
#pragma unroll
        for (int ks = 0; ks < 14; ++ks) xf[ks] = LD16(rr, lane * 16, (s * 112 + 14 * w + ks) * 1024, SC1);
        u32x2 hr = {0u, 0u};
        if (w < 4) hr = LD8(rh, lane * 16 + (w & 1) * 8, s * (int)SZ_SEGF + (2 * ctile + (w >> 1)) * 1024, SC1);
        f32x16 acc;
#pragma unroll
        for (int i = 0; i < 16; ++i) acc[i] = 0.f;
#pragma unroll
        for (int ks = 0; ks < 14; ++ks) acc = mfma32(wf[ks], xf[ks], acc);
#pragma unroll
        for (int g = 0; g < 4; ++g) *(f32x4*)(smem + ((g * 8 + w) * 64 + lane) * 16) = (f32x4){acc[4 * g], acc[4 * g + 1], acc[4 * g + 2], acc[4 * g + 3]};
        BAR();
        if (w < 4) {
            f32x4 t[8];
#pragma unroll
            for (int k = 0; k < 8; ++k) t[k] = *(const f32x4*)(smem + ((w * 8 + k) * 64 + lane) * 16);
            const f32x4 o = ((t[0] + t[1]) + (t[2] + t[3])) + ((t[4] + t[5]) + (t[6] + t[7]));
            const float bv[4] = {bf_lo(br.x), bf_hi(br.x), bf_lo(br.y), bf_hi(br.y)};
            const float hv[4] = {bf_lo(hr.x), bf_hi(hr.x), bf_lo(hr.y), bf_hi(hr.y)};
            float y[4];
#pragma unroll
            for (int e = 0; e < 4; ++e) y[e] = hv[e] + rbf(rbf(o[e]) + bv[e]);
            *(u32x2*)(p.out + (size_t)(32 * s + tok) * DM + 32 * ctile + 8 * w + 4 * hh) = (u32x2){pk_bf(y[0], y[1]), pk_bf(y[2], y[3])};
        }
    }
}

constexpr int L_F = 0;
constexpr int L_R = 14336;
constexpr int L_MX = L_R + 1792;
constexpr int L_R2 = L_MX + 128;

constexpr unsigned POLL_CAP = 1u << 14;
#define NOCOMM 0
DI u32x4 poll16(rsrc_t r, int vo, int so, unsigned* ctrl, unsigned& cap, unsigned code = 1u, unsigned* np = nullptr) {
    u32x4 v;
    unsigned sp = 0;
    bool bad;
    do {
        asm volatile("" ::: "memory");
        v = LD16(r, vo, so, SC1);
        bad = __any(max(max(v.x, v.y), max(v.z, v.w)) == 0xffffffffu);
    } while (bad && ++sp < cap);
    if (np) *np = sp;
    if (bad) { st_agent(ctrl + CT_ABORT, 1u); cap = 1u; }
    return v;
}
DI float fmax_raw(float a, float b) { float r; asm("v_max_f32 %0, %1, %2" : "=v"(r) : "v"(a), "v"(b)); return r; }
DI float fmax3_raw(float a, float b, float c) { float r; asm("v_max3_f32 %0, %1, %2, %3" : "=v"(r) : "v"(a), "v"(b), "v"(c)); return r; }

#define DELAY_X 7
#define DELAY_Y 0
#define K_Y 136
#define K_X 236
#define T_START 30
#define D_Y 25
#define D_X 25
DI unsigned later(unsigned a, unsigned b) { return (int)(a - b) >= 0 ? a : b; }
DI unsigned now_ticks() { return (unsigned)__builtin_amdgcn_s_memrealtime(); }
DI void wait_until(unsigned target) {
    unsigned long long t;
    unsigned d;
    asm volatile("s_memrealtime %0\n\ts_waitcnt lgkmcnt(0)" : "=s"(t) :: "memory");
    d = target - (unsigned)t;
    if (d - 1u < 4000u) {
        do {
            asm volatile("s_memrealtime %0\n\ts_waitcnt lgkmcnt(0)" : "=s"(t) :: "memory");
        } while ((int)(target - (unsigned)t) > 0);
    }
}
constexpr int L_TX = L_R2 + 4096;

template <bool BROLE, bool W7>
DI void rec_wave(const Params& p, char* smem, const int c, const int w) {
    const int tid = threadIdx.x, lane = tid & 63;
    const int x = lane & 15, q = lane >> 4, ap = (lane >> 3) & 1, i8 = lane & 7;
    const int h = c >> 2, c4 = c & 3;
    const int u = c >= 28 ? 1 : 0, J = c - 28 * u, g = 8 * u + w;
    unsigned* ctrl = (unsigned*)(p.ws + OFF_CTRL);
    const rsrc_t rhx = mk_rsrc(p.hx, (unsigned)SZ_HX), rkt = mk_rsrc(p.ws + OFF_KT, (unsigned)SZ_KT), rvt = mk_rsrc(p.ws + OFF_VT, (unsigned)SZ_KT);
    const rsrc_t rkm = mk_rsrc(p.ws + OFF_KM, (unsigned)SZ_KM), rvm = mk_rsrc(p.ws + OFF_VM, (unsigned)SZ_KM);
    unsigned cap = POLL_CAP;

    u32x4 wA[14], wB[14];
    auto load_w = [&](u32x4 (&wt)[14], int np) {
        const char* b = p.ws + OFF_WCT + ((size_t)(np >> 5) * 56 * 32 + q * 32 + (np & 31)) * 16;
#pragma unroll
        for (int ks = 0; ks < 14; ++ks) wt[ks] = *(const u32x4*)(b + ks * 2048);
    };
    if (!W7) load_w(wA, h * 448 + 112 * c4 + 16 * w + x);
    if (BROLE) load_w(wB, 7168 + g * 448 + 16 * J + x);
    if (!W7 && w == 0) {
        unsigned best0 = ~0u, best1 = ~0u;
        for (int rep = 0; rep < 6; ++rep) {
            const int off = c * 65536 + 16384 + (rep & 1) * 8192 + 8064;
            const unsigned long long t0 = __builtin_amdgcn_s_memtime();
            ST4((unsigned)rep, rhx, 0, off, SC1);
            asm volatile("s_waitcnt vmcnt(0)" ::: "memory");
            unsigned v = __builtin_amdgcn_raw_buffer_load_b32(rhx, 0, off, SC1);
            asm volatile("s_waitcnt vmcnt(0)" : "+v"(v) :: "memory");
            const unsigned dt = (unsigned)(__builtin_amdgcn_s_memtime() - t0);
            if (rep & 1) best1 = min(best1, dt); else best0 = min(best0, dt);
        }
        if (lane == 0) st_agent(ctrl + CT_PROBE(c), 2u | (best1 < best0 ? 1u : 0u));
        unsigned long long pr = 0;
        for (unsigned sp = 0; sp < SPIN_CAP; ++sp) {
            const unsigned v = ld_agent(ctrl + CT_PROBE(lane));
            pr = __ballot((v & 1u) != 0u);
            if (!__any(v == 0u)) break;
            __builtin_amdgcn_s_sleep(4);
        }
        const int cnt = __builtin_popcountll((pr >> (lane & ~7)) & 0xffull);
        pr = __ballot(cnt > 4 || (cnt == 4 && ((pr >> lane) & 1ull)));
        if (lane == 0) *(LAS unsigned long long*)(smem + L_TX + 8) = pr;
    }
    if (!W7) {
        const int o = tid >> 3, i = tid & 7;
        *(u32x4*)(smem + L_F + (o * 16 + i) * 16) = *(const u32x4*)(p.mem0 + i * DM + o * 8);
        *(u32x4*)(smem + L_F + (o * 16 + 8 + i) * 16) = (u32x4){0u, 0u, 0u, 0u};
    }
    BAR();
    unsigned long long probe;
    {
        const u32x2 pv = *(LAS u32x2*)(smem + L_TX + 8);
        probe = ((unsigned long long)(unsigned)__builtin_amdgcn_readfirstlane(pv.y) << 32) | (unsigned)__builtin_amdgcn_readfirstlane(pv.x);
    }
    {
        const u32x4 ones = {~0u, ~0u, ~0u, ~0u};
        const int so = c * 65536 + (int)(((probe >> c) & 1ull) << 13);
#pragma unroll
        for (int i = 0; i < 4; ++i) ST16(ones, rhx, i * 16384 + tid * 16, so, SC1);
        asm volatile("s_waitcnt vmcnt(0)" ::: "memory");
        BAR();
        if (!W7 && w == 0) {
            if (lane == 0) add_agent(ctrl + CT_START(c & 7), 1u);
            wait_shards(ctrl, CT_KV(0, 0), KV_PER_SHARD, 0);
            wait_shards(ctrl, CT_KV(1, 0), KV_PER_SHARD, 0);
            for (unsigned sp = 0; sp < SPIN_CAP; ++sp) {
                if (!__any(ld_agent(ctrl + CT_START(lane & 7)) < 8u)) break;
                __builtin_amdgcn_s_sleep(4);
            }
        }
        BAR();
    }
    STAMP(c, w);
    auto phys = [](int lo) { return ((lo >> 13) << 14) + (lo & 8191); };
    const int vo_x0 = phys(tid * 16), vo_x1 = phys(7168 + tid * 16);
    const int vo_yl = phys(16384 + (g * 4 + 2 * ap + (q >> 1)) * 256 + i8 * 32 + (q & 1) * 16);
    const int vo_ys = phys(16384 + c * 256 + lane * 4);
    const int vo_xs = phys(u * 7168 + J * 256 + lane * 4);
    const int vo_ts = phys(14336 + c * 16), vo_tl = phys(14336 + lane * 16);
    const int vo_k = (q * 32 + 8 * c4 + i8) * 16;
    const int vo_v = ((q & 1) * 32 + 16 * (J & 1) + x) * 16 + 8 * (q >> 1);
    const int vo_km = ((14 * c4 + 2 * w + (q >> 1)) * 8 + i8) * 16 + (q & 1) * 8;
    const int vo_vm = (16 * J + x) * 16 + (q & 1) * 8;
    const int scol = 112 * c4 + 16 * w + 4 * q;
    const int lo_s = L_F + ((scol >> 3) * 16 + x) * 16 + (scol & 4) * 2;
    const int lo_f = L_F + ((tid >> 3) * 16 + (tid & 7)) * 16;
    auto step_off = [&](int s) { return s * 65536 + (int)(((probe >> s) & 1ull) << 13); };

    u32x2 v1 = {0u, 0u}, v2 = {0u, 0u}, vn1 = {0u, 0u}, vn2 = {0u, 0u};
    auto v_load = [&](int s) {
        const int so = ((s * 16 + g) * 14 + (J >> 1)) * 2048;
        vn1 = LD8(rvt, vo_v, so, SC1); vn2 = LD8(rvt, vo_v + 1024, so, SC1);
    };
    auto k_load = [&](int s) {
#pragma unroll
        for (int ks = 0; ks < 14; ++ks) wA[ks] = LD16(rkt, vo_k, (s * 16 + h) * 28672 + ks * 2048, SC1);
    };
    if (BROLE) v_load(0);
    if (W7) k_load(0);
    u32x4 fr[14];
    int kv_ready = 2;
    const unsigned long long m_ap = __ballot(ap != 0);
    const unsigned hm = (q >> 1) ? 0xffff0000u : 0x0000ffffu, m_a = ap ? 0u : hm, m_b = ap ? hm : 0u;
    const unsigned HI = 0xffff0000u;
    const float L2E = LOG2E, NL2E = -LOG2E;
#define SB() __builtin_amdgcn_sched_barrier(0)
    auto gather_t = [&](int s, int so_s, int n) {
        unsigned t, sp = 0;
        bool bad;
        do {
            asm volatile("" ::: "memory");
            t = __builtin_amdgcn_raw_buffer_load_b32(rhx, n > 1 ? vo_tl : phys(14336), so_s, SC1);
            bad = __any(lane < n && t == 0xffffffffu);
        } while (bad && ++sp < cap);
        if (bad) { st_agent(ctrl + CT_ABORT, 1u); cap = 1u; }
        const unsigned ref = __builtin_amdgcn_readfirstlane(t);
        int d = lane < n ? (int)(t - ref) : -0x7fffffff;
        d = max(d, __builtin_amdgcn_update_dpp(d, d, 0x128, 0xf, 0xf, false));
        d = max(d, __builtin_amdgcn_update_dpp(d, d, 0x124, 0xf, 0xf, false));
        d = max(d, __builtin_amdgcn_update_dpp(d, d, 0x122, 0xf, 0xf, false));
        d = max(d, __builtin_amdgcn_update_dpp(d, d, 0x121, 0xf, 0xf, false));
        { auto r = __builtin_amdgcn_permlane16_swap((unsigned)d, (unsigned)d, false, false); d = max((int)r[0], (int)r[1]); }
        { auto r = __builtin_amdgcn_permlane32_swap((unsigned)d, (unsigned)d, false, false); d = max((int)r[0], (int)r[1]); }
        if (lane == 0) *(LAS unsigned*)(smem + L_TX + (s & 1) * 4) = ref + (unsigned)d;
    };
    auto publish_t = [&](int so_s) { if (lane == 0) ST4(min(now_ticks(), 0xfffffffeu), rhx, vo_ts, so_s, SC1); };
    auto gather_x = [&](int s, int so_s, unsigned t_issue) {
        wait_until(t_issue);
        RSTAMP(1, (s - 1) * 16 + 9);
        unsigned sp = 0;
        auto pass = [&]() {
            asm volatile("" ::: "memory");
            const u32x4 a0 = LD16(rhx, vo_x0, so_s, SC1), a1 = LD16(rhx, vo_x1, so_s, SC1);
            *(u32x4*)(smem + lo_f) = a0;
            *(u32x4*)(smem + lo_f + 128) = a1;
            return NOCOMM == 0 && __any(max(max(max(a0.x, a0.y), max(a0.z, a0.w)), max(max(a1.x, a1.y), max(a1.z, a1.w))) == 0xffffffffu);
        };
        if (__builtin_expect(pass(), 0)) {
            bool bad = true;
            while (bad && ++sp < cap) bad = pass();
            if (bad) { st_agent(ctrl + CT_ABORT, 1u); cap = 1u; }
        }
        RVAL(1, (s - 1) * 16 + 12, sp + 1);
        RSTAMP(1, (s - 1) * 16 + 10);
    };
    auto a_mfma = [&]() {
#pragma unroll
        for (int ks = 0; ks < 4; ++ks) fr[ks] = *(const u32x4*)(smem + L_F + ks * 1024 + lane * 16);
        SB();
        f32x4 acc;
        asm volatile("v_mfma_f32_16x16x32_bf16 %0, %1, %2, 0" : "=&v"(acc) : "v"(wA[0]), "v"(fr[0]));
        fr[4] = *(const u32x4*)(smem + L_F + 4 * 1024 + lane * 16);
        SB();
#pragma unroll
        for (int ks = 1; ks < 14; ++ks) {
            asm volatile("v_mfma_f32_16x16x32_bf16 %0, %1, %2, %0" : "+v"(acc) : "v"(wA[ks]), "v"(fr[ks]));
            if (ks + 4 < 14) fr[ks + 4] = *(const u32x4*)(smem + L_F + (ks + 4) * 1024 + lane * 16);
            SB();
        }
        float a0, a1, a2, a3;
        asm volatile("s_nop 7\n\t"
                     "v_add_f32_dpp %0, %4, %4 row_ror:8 row_mask:0xf bank_mask:0xf\n\tv_add_f32_dpp %1, %5, %5 row_ror:8 row_mask:0xf bank_mask:0xf\n\t"
                     "v_add_f32_dpp %2, %6, %6 row_ror:8 row_mask:0xf bank_mask:0xf\n\tv_add_f32_dpp %3, %7, %7 row_ror:8 row_mask:0xf bank_mask:0xf"
                     : "=&v"(a0), "=&v"(a1), "=&v"(a2), "=&v"(a3) : "v"(acc[0]), "v"(acc[1]), "v"(acc[2]), "v"(acc[3]));
        return (u32x2){pk_bf(a0, a1), pk_bf(a2, a3)};
    };
    auto u_part = [&]() {
        f32x4 ua = {0.f, 0.f, 0.f, 0.f};
#pragma unroll
        for (int ks = 0; ks < 14; ++ks) ua = mfma16(fr[ks], wB[ks], ua);
#pragma unroll
        for (int r = 0; r < 4; ++r) ua[r] = xsum32(ua[r]);
        return (u32x2){pk_bf(ua[0], ua[1]), pk_bf(ua[2], ua[3])};
    };

    int so_s = step_off(0);
    if (W7) {
        if (c == 0 && lane == 0) ST4(min(now_ticks() - T_START, 0xfffffffeu), rhx, vo_ts, so_s, SC1);
        gather_t(0, so_s, 1);
    }
    for (int s = 0; s < NSEG - 1; ++s) {
        BAR();
        RSTAMP(1, s * 16 + 0);
        if (BROLE) {
            v1 = vn1; v2 = vn2;
            v_load(s + 1);
        }
        u32x2 tb;
        if (!W7) {
            const u32x2 sop = *(const u32x2*)(smem + lo_s);
            tb = a_mfma();
            f32x4 st;
            float s0, s1, s2, s3;
            asm volatile("s_nop 1\n\tv_mfma_f32_16x16x16_bf16 %0, %1, %2, 0" : "=&v"(st) : "v"(tb), "v"(sop));
            asm volatile("s_nop 7\n\t"
                         "v_add_f32_dpp %0, %4, %4 row_ror:8 row_mask:0xf bank_mask:0xf\n\tv_add_f32_dpp %1, %5, %5 row_ror:8 row_mask:0xf bank_mask:0xf\n\t"
                         "v_add_f32_dpp %2, %6, %6 row_ror:8 row_mask:0xf bank_mask:0xf\n\tv_add_f32_dpp %3, %7, %7 row_ror:8 row_mask:0xf bank_mask:0xf"
                         : "=&v"(s0), "=&v"(s1), "=&v"(s2), "=&v"(s3) : "v"(st[0]), "v"(st[1]), "v"(st[2]), "v"(st[3]));
            RSTAMP(1, s * 16 + 1);
            if (x < 8 && q < 2) *(f32x4*)(smem + L_R + (w * 16 + 2 * x + q) * 16) = (f32x4){s0, s1, s2, s3};
        } else {
            tb = a_mfma();
            if (x < 8 && q < 2) *(u32x2*)(smem + L_MX + (2 * x + q) * 8) = tb;
        }
        BAR();
        const unsigned t_b2 = now_ticks();
        RSTAMP(1, s * 16 + 2);
        if (!W7 && w == 0) {
            const int chunk = lane >> 2, part = lane & 3;
            f32x2 t[7];
#pragma unroll
            for (int i = 0; i < 7; ++i) t[i] = *(const f32x2*)(smem + L_R + (i * 16 + chunk) * 16 + (part & 1) * 8);
            unsigned mxv = *(const unsigned*)(smem + L_MX + chunk * 8 + (part & 1) * 4);
            asm volatile("" : "+v"(mxv));
            const f32x2 sum = ((t[0] + t[1]) + (t[2] + t[3])) + ((t[4] + t[5]) + t[6]);
            unsigned mmv = pk_bf(sum[0], sum[1]);
            asm volatile("" : "+v"(mmv));
            ST4(min(part < 2 ? mxv : mmv, 0xfffffffeu), rhx, vo_ys, so_s, SC1);
            RSTAMP(0, s * 16 + 3); RVAL(0, s * 16 + 14, (unsigned)__builtin_amdgcn_s_memrealtime());
        }
        SB();
        const unsigned t_prev = __builtin_amdgcn_readfirstlane(*(LAS unsigned*)(smem + L_TX + (s & 1) * 4));
        const int so_n = step_off(s + 1);
        if (!W7 && s > 0 && w == 3 && tid_opaque() == 192) add_agent(ctrl + CT_M(s - 1, c & 7), 1u);
        if (!W7 && x < 8) ST8(tb, rkm, vo_km, (s * 16 + h) * 7168, SC1);
        if (W7) k_load(s + 1);
        unsigned t_x = t_prev + K_X;
        if (BROLE) {
            RSTAMP(1, s * 16 + 4);
            wait_until(later(t_prev + K_Y, t_b2 + D_Y));
            RSTAMP(1, s * 16 + 5);
            asm volatile("" ::: "memory");
            u32x4 y = LD16(rhx, vo_yl, so_s, SC1);
            SB();
            const u32x2 ub = u_part();
            SB();
            unsigned sp = 0;
            float o0, o1_, o2_, o3;
            auto pass = [&]() {
                unsigned long long bad;
                float x0, x1, x2, x3, sm, p0, p1, p2, p3, m, t;
                asm volatile(
                    "v_lshlrev_b32 %[p0], 16, %[y2]\n\tv_and_b32 %[p1], %[hi], %[y2]\n\tv_lshlrev_b32 %[p2], 16, %[y3]\n\tv_and_b32 %[p3], %[hi], %[y3]\n\t"
                    "v_lshlrev_b32 %[x0], 16, %[y0]\n\tv_and_b32 %[x1], %[hi], %[y0]\n\t"
                    "v_permlane32_swap_b32 %[p0], %[p1]\n\tv_permlane32_swap_b32 %[p2], %[p3]\n\t"
                    "v_lshlrev_b32 %[x2], 16, %[y1]\n\tv_and_b32 %[x3], %[hi], %[y1]\n\t"
                    "v_add_f32 %[p0], %[p0], %[p1]\n\tv_add_f32 %[p2], %[p2], %[p3]\n\t"
                    "v_max3_u32 %[t], %[y0], %[y1], %[y2]\n\t"
                    "v_cndmask_b32 %[p1], %[p2], %[p0], %[ap]\n\tv_cndmask_b32 %[p3], %[p0], %[p2], %[ap]\n\t"
                    "v_max3_f32 %[m], %[x0], %[x1], %[x2]\n\t"
                    "v_max_u32 %[t], %[t], %[y3]\n\t"
                    "v_add_f32_dpp %[sm], %[p1], %[p3] row_ror:8 row_mask:0xf bank_mask:0xf\n\t"
                    "v_cmp_eq_u32 %[bad], -1, %[t]\n\t"
                    "v_max3_f32 %[m], %[m], %[x3], %[sm]\n\t"
                    "s_nop 1\n\t"
                    "v_max_f32_dpp %[m], %[m], %[m] row_ror:8 row_mask:0xf bank_mask:0xf\n\t"
                    "v_mov_b32 %[t], %[m]\n\ts_nop 1\n\tv_permlane16_swap_b32 %[m], %[t]\n\tv_max_f32 %[m], %[m], %[t]\n\t"
                    "v_mov_b32 %[t], %[m]\n\ts_nop 1\n\tv_permlane32_swap_b32 %[m], %[t]\n\tv_max_f32 %[m], %[m], %[t]\n\t"
                    "v_mul_f32 %[m], %[nl2e], %[m]\n\t"
                    "v_fma_f32 %[sm], %[sm], %[l2e], %[m]\n\tv_fma_f32 %[x0], %[x0], %[l2e], %[m]\n\tv_fma_f32 %[x1], %[x1], %[l2e], %[m]\n\t"
                    "v_fma_f32 %[x2], %[x2], %[l2e], %[m]\n\tv_fma_f32 %[x3], %[x3], %[l2e], %[m]\n\t"
                    "v_exp_f32 %[sm], %[sm]\n\tv_exp_f32 %[x0], %[x0]\n\tv_exp_f32 %[x1], %[x1]\n\tv_exp_f32 %[x2], %[x2]\n\tv_exp_f32 %[x3], %[x3]\n\ts_nop 0"
                    : [x0] "=&v"(x0), [x1] "=&v"(x1), [x2] "=&v"(x2), [x3] "=&v"(x3), [sm] "=&v"(sm), [p0] "=&v"(p0), [p1] "=&v"(p1), [p2] "=&v"(p2), [p3] "=&v"(p3),
                      [m] "=&v"(m), [t] "=&v"(t), [bad] "=&s"(bad)
                    : [y0] "v"(y.x), [y1] "v"(y.y), [y2] "v"(y.z), [y3] "v"(y.w), [hi] "s"(HI), [ap] "s"(m_ap), [l2e] "s"(L2E), [nl2e] "s"(NL2E));
                const unsigned tm = pk_bf(sm, sm);
                const u32x2 pb = {pk_bf(x0, x1), pk_bf(x2, x3)};
                const u32x2 pm = {tm & m_a, tm & m_b};
                f32x4 om, oa, ob;
                asm volatile("s_nop 1\n\tv_mfma_f32_16x16x16_bf16 %0, %3, %4, 0\n\tv_mfma_f32_16x16x16_bf16 %1, %5, %7, %0\n\tv_mfma_f32_16x16x16_bf16 %2, %6, %7, %0"
                             : "=&v"(om), "=&v"(oa), "=&v"(ob) : "v"(ub), "v"(pm), "v"(v1), "v"(v2), "v"(pb));
                float l, l2;
                asm volatile(
                    "v_add_f32 %[l], %[x0], %[x1]\n\tv_add_f32 %[l2], %[x2], %[x3]\n\tv_add_f32 %[l], %[l], %[sm]\n\tv_add_f32 %[l], %[l], %[l2]\n\ts_nop 1\n\t"
                    "v_add_f32_dpp %[l], %[l], %[l] row_ror:8 row_mask:0xf bank_mask:0xf\n\t"
                    "v_mov_b32 %[l2], %[l]\n\ts_nop 1\n\tv_permlane16_swap_b32 %[l], %[l2]\n\tv_add_f32 %[l], %[l], %[l2]\n\t"
                    "v_mov_b32 %[l2], %[l]\n\ts_nop 1\n\tv_permlane32_swap_b32 %[l], %[l2]\n\tv_add_f32 %[l], %[l], %[l2]\n\t"
                    "v_rcp_f32 %[l], %[l]\n\t"
                    "v_add_f32_dpp %[r0], %[b0], %[a0] row_ror:8 row_mask:0xf bank_mask:0xf\n\tv_add_f32_dpp %[r1], %[b1], %[a1] row_ror:8 row_mask:0xf bank_mask:0xf\n\t"
                    "v_add_f32_dpp %[r2], %[b2], %[a2] row_ror:8 row_mask:0xf bank_mask:0xf\n\tv_add_f32_dpp %[r3], %[b3], %[a3] row_ror:8 row_mask:0xf bank_mask:0xf\n\t"
                    "v_mul_f32 %[r0], %[r0], %[l]\n\tv_mul_f32 %[r1], %[r1], %[l]\n\tv_mul_f32 %[r2], %[r2], %[l]\n\tv_mul_f32 %[r3], %[r3], %[l]"
                    : [l] "=&v"(l), [l2] "=&v"(l2), [r0] "=&v"(o0), [r1] "=&v"(o1_), [r2] "=&v"(o2_), [r3] "=&v"(o3)
                    : [x0] "v"(x0), [x1] "v"(x1), [x2] "v"(x2), [x3] "v"(x3), [sm] "v"(sm),
                      [a0] "v"(oa[0]), [a1] "v"(oa[1]), [a2] "v"(oa[2]), [a3] "v"(oa[3]), [b0] "v"(ob[0]), [b1] "v"(ob[1]), [b2] "v"(ob[2]), [b3] "v"(ob[3]));
                return NOCOMM == 0 && bad != 0;
            };
            if (__builtin_expect(pass(), 0)) {
                bool bad = true;
                while (bad && ++sp < cap) { asm volatile("" ::: "memory"); y = LD16(rhx, vo_yl, so_s, SC1); bad = pass(); }
                if (bad) { st_agent(ctrl + CT_ABORT, 1u); cap = 1u; }
            }
            const f32x4 o = {o0, o1_, o2_, o3};
            RSTAMP(1, s * 16 + 6);
            if (x < 8) *(f32x4*)(smem + L_R2 + ((w * 8 + x) * 4 + q) * 16) = o;
            BAR();
            t_x = later(t_x, now_ticks() + D_X);
            RSTAMP(1, s * 16 + 7);
            if (W7) {
                const int hf = lane >> 5, i = (lane >> 2) & 7, d = lane & 3;
                f32x2 t[8];
#pragma unroll
                for (int k = 0; k < 8; ++k) t[k] = *(const f32x2*)(smem + L_R2 + ((k * 8 + i) * 4 + 2 * hf + (d >> 1)) * 16 + (d & 1) * 8);
                const f32x2 sum = ((t[0] + t[1]) + (t[2] + t[3])) + ((t[4] + t[5]) + (t[6] + t[7]));
                ST4(min(pk_bf(sum[0], sum[1]), 0xfffffffeu), rhx, vo_xs, so_n, SC1);
                if (c == 0) publish_t(so_n);
                RSTAMP(7, s * 16 + 8); RVAL(7, s * 16 + 15, (unsigned)__builtin_amdgcn_s_memrealtime());
            }
            SB();
            if (q < 2) ST8(ub, rvm, vo_vm, (s * 16 + g) * 7168, SC1);
            RVAL(1, s * 16 + 11, sp + 1);
        }
        if (W7) {
            if (s + 2 < NSEG && s + 2 >= kv_ready) {
                for (unsigned sp = 0; sp < SPIN_CAP; ++sp) {
                    const int sg = s + 2 + (lane >> 3);
                    const unsigned long long rdy = __ballot(sg >= NSEG || ld_agent(ctrl + CT_KV(sg & 63, lane & 7)) >= KV_PER_SHARD);
                    kv_ready = s + 2 + ((rdy == ~0ull ? 64 : __builtin_ctzll(~rdy)) >> 3);
                    if (s + 2 < kv_ready) break;
                    __builtin_amdgcn_s_sleep(4);
                    if (sp + 1 == SPIN_CAP) st_agent(ctrl + CT_ABORT, 1u);
                }
            }
            wait_until(t_x);
            gather_t(s + 1, so_n, 1);
        } else {
            gather_x(s + 1, so_n, t_x);
        }
        so_s = so_n;
    }
    {
        const int s = NSEG - 1;
        BAR();
        if (!W7 && tid_opaque() == 0) add_agent(ctrl + CT_M(s - 1, c & 7), 1u);
        if (!W7) {
            const u32x2 tb = a_mfma();
            if (x < 8) ST8(tb, rkm, vo_km, (s * 16 + h) * 7168, SC1);
        } else {
#pragma unroll
            for (int ks = 0; ks < 14; ++ks) fr[ks] = *(const u32x4*)(smem + L_F + ks * 1024 + lane * 16);
        }
        if (BROLE) { const u32x2 ub = u_part(); if (q < 2) ST8(ub, rvm, vo_vm, (s * 16 + g) * 7168, SC1); }
        asm volatile("s_waitcnt vmcnt(0)" ::: "memory");
        BAR();
        if (!W7 && tid_opaque() == 0) add_agent(ctrl + CT_M(s, c & 7), 1u);
    }
}

DI void rec_role(const Params& p, char* smem, int c) {
    const int w = __builtin_amdgcn_readfirstlane(threadIdx.x >> 6);
    if (c < 56) { if (w < 7) rec_wave<true, false>(p, smem, c, w); else rec_wave<true, true>(p, smem, c, w); }
    else        { if (w < 7) rec_wave<false, false>(p, smem, c, w); else rec_wave<false, true>(p, smem, c, w); }
}

constexpr int LA_X = 0;
constexpr int LA_SM = 28672;
constexpr int LA_PX = LA_SM + 16384;
constexpr int LA_PM = LA_PX + 8192;
constexpr int LA_RED = LA_PM + 2048;
constexpr int LA_BC = LA_RED + 1024;

DI void attn_item(const Params& p, char* smem, int s, int hg) {
    const int tid = threadIdx.x, lane = tid & 63, w = __builtin_amdgcn_readfirstlane(tid >> 6);
    const int tok = lane & 31, hh = lane >> 5;
    unsigned* ctrl = (unsigned*)(p.ws + OFF_CTRL);
    const rsrc_t rkt = mk_rsrc(p.ws + OFF_KT, (unsigned)SZ_KT), rvt = mk_rsrc(p.ws + OFF_VT, (unsigned)SZ_KT);
    const rsrc_t rkm = mk_rsrc(p.ws + OFF_KM, (unsigned)SZ_KM), rvm = mk_rsrc(p.ws + OFF_VM, (unsigned)SZ_KM);
    const rsrc_t rop = mk_rsrc(p.ws + OFF_OP, (unsigned)SZ_OP);
    const rsrc_t rhd = mk_rsrc(p.ws + OFF_HD, (unsigned)(64 * SZ_SEGF)), rhn = mk_rsrc(p.ws + OFF_HNF, (unsigned)(64 * SZ_SEGF));
    {
        const char* src = p.ws + OFF_XNF + (size_t)s * SZ_SEGF;
#pragma unroll
        for (int i = 0; i < 4; ++i) { const int k = w + 8 * i; if (k < 28) GLDS(src + k * 1024 + lane * 16, smem + LA_X + k * 1024, 0); }
    }
    if (w == 0) wait_shards(ctrl, CT_KV(s, 0), KV_PER_SHARD, 2);
    asm volatile("s_waitcnt vmcnt(0)" ::: "memory");
    BAR();
    const int nt = w < 6 ? 2 : 1;
    f32x16 sx;
#pragma unroll
    for (int i = 0; i < 16; ++i) sx[i] = 0.f;
    if (w < 4) {
        u32x4 kf[28];
#pragma unroll
        for (int ks = 0; ks < 28; ++ks) kf[ks] = LD16(rkt, lane * 16, (s * 16 + 4 * hg + w) * 28672 + ks * 1024, SC1);
#pragma unroll
        for (int ks = 0; ks < 28; ++ks) sx = mfma32(kf[ks], *(const u32x4*)(smem + LA_X + ks * 1024 + lane * 16), sx);
    }
    u32x4 vf[2][8];
#pragma unroll
    for (int t = 0; t < 2; ++t)
        if (t < nt) {
#pragma unroll
            for (int k = 0; k < 8; ++k) vf[t][k] = LD16(rvt, lane * 16, (((s * 16 + 4 * hg + (k >> 1)) * 14 + w + 8 * t) * 2 + (k & 1)) * 1024, SC1);
        }
    if (w == 4) wait_shards(ctrl, CT_M(s, 0), M_PER_SHARD, s < 60 ? 4 : 1);
    BAR();
    u32x2 vm[2][4];
#pragma unroll
    for (int t = 0; t < 2; ++t)
        if (t < nt) {
#pragma unroll
            for (int k = 0; k < 4; ++k) vm[t][k] = LD8(rvm, (32 * (w + 8 * t) + tok) * 16 + hh * 8, (s * 16 + 4 * hg + k) * 7168, SC1);
        }
    if (w >= 4) {
        const int kq = w - 4;
        u32x4 mf[7];
#pragma unroll
        for (int i = 0; i < 7; ++i) mf[i] = LD16(rkm, (hh * 8 + (tok & 7)) * 16 + (tok >> 3) * 7168, (s * 16 + 4 * hg) * 7168 + (7 * kq + i) * 256, SC1);
        f32x16 sm;
#pragma unroll
        for (int i = 0; i < 16; ++i) sm[i] = 0.f;
#pragma unroll
        for (int i = 0; i < 7; ++i) sm = mfma32(mf[i], *(const u32x4*)(smem + LA_X + (7 * kq + i) * 1024 + lane * 16), sm);
#pragma unroll
        for (int gq = 0; gq < 4; ++gq) *(f32x4*)(smem + LA_SM + ((kq * 4 + gq) * 64 + lane) * 16) = (f32x4){sm[4 * gq], sm[4 * gq + 1], sm[4 * gq + 2], sm[4 * gq + 3]};
    }
    BAR();
    if (w < 4) {
        f32x4 m4 = *(const f32x4*)(smem + LA_SM + ((0 * 4 + w) * 64 + lane) * 16);
#pragma unroll
        for (int kq = 1; kq < 4; ++kq) m4 += *(const f32x4*)(smem + LA_SM + ((kq * 4 + w) * 64 + lane) * 16);
        float mx = fmaxf(fmaxf(m4[0], m4[1]), fmaxf(m4[2], m4[3]));
#pragma unroll
        for (int i = 0; i < 16; ++i) {
            const int key = (i & 3) + 8 * (i >> 2) + 4 * hh;
            sx[i] = key <= tok ? sx[i] : -1e30f;
            mx = fmaxf(mx, sx[i]);
        }
        mx = xmax32(mx);
        const float mc = mx * LOG2E;
        float l = 0.f;
#pragma unroll
        for (int i = 0; i < 16; ++i) { sx[i] = __builtin_amdgcn_exp2f(sx[i] * LOG2E - mc); l += sx[i]; }
#pragma unroll
        for (int i = 0; i < 4; ++i) { m4[i] = __builtin_amdgcn_exp2f(m4[i] * LOG2E - mc); l += m4[i]; }
        l = xsum32(l);
        const float inv = 1.0f / l;
#pragma unroll
        for (int k = 0; k < 2; ++k) {
            u32x4 v = {pk_bf(sx[8 * k] * inv, sx[8 * k + 1] * inv), pk_bf(sx[8 * k + 2] * inv, sx[8 * k + 3] * inv),
                       pk_bf(sx[8 * k + 4] * inv, sx[8 * k + 5] * inv), pk_bf(sx[8 * k + 6] * inv, sx[8 * k + 7] * inv)};
            *(u32x4*)(smem + LA_PX + ((w * 2 + k) * 64 + lane) * 16) = v;
        }
        *(u32x2*)(smem + LA_PM + (w * 64 + lane) * 8) = (u32x2){pk_bf(m4[0] * inv, m4[1] * inv), pk_bf(m4[2] * inv, m4[3] * inv)};
    }
    BAR();
#pragma unroll
    for (int t = 0; t < 2; ++t)
        if (t < nt) {
            f32x16 o;
#pragma unroll
            for (int i = 0; i < 16; ++i) o[i] = 0.f;
#pragma unroll
            for (int k = 0; k < 8; ++k) o = mfma32(vf[t][k], *(const u32x4*)(smem + LA_PX + (k * 64 + lane) * 16), o);
#pragma unroll
            for (int pp = 0; pp < 2; ++pp) {
                const u32x2 pa = *(const u32x2*)(smem + LA_PM + ((2 * pp) * 64 + lane) * 8), pb = *(const u32x2*)(smem + LA_PM + ((2 * pp + 1) * 64 + lane) * 8);
                o = mfma32((u32x4){vm[t][2 * pp].x, vm[t][2 * pp].y, vm[t][2 * pp + 1].x, vm[t][2 * pp + 1].y}, (u32x4){pa.x, pa.y, pb.x, pb.y}, o);
            }
#pragma unroll
            for (int gq = 0; gq < 4; ++gq) {
                f32x4 v = {o[4 * gq], o[4 * gq + 1], o[4 * gq + 2], o[4 * gq + 3]};
                ST16(__builtin_bit_cast(u32x4, v), rop, lane * 16 + gq * 1024, ((s * 4 + hg) * 14 + w + 8 * t) * 4096, SC1);
            }
        }
    asm volatile("s_waitcnt vmcnt(0)" ::: "memory");
    BAR();
    if (tid_opaque() == 0) *(volatile LAS unsigned*)(smem + LA_BC) = add_agent(ctrl + CT_A(s), 1u);
    BAR();
    const unsigned arrived = __builtin_amdgcn_readfirstlane(*(volatile LAS unsigned*)(smem + LA_BC));
    if (arrived == 3u) {
        float hv[2][16];
        float ssum = 0.f;
#pragma unroll
        for (int t = 0; t < 2; ++t)
            if (t < nt) {
#pragma unroll
                for (int gq = 0; gq < 4; ++gq) {
                    f32x4 a = __builtin_bit_cast(f32x4, LD16(rop, lane * 16 + gq * 1024, ((s * 4 + 0) * 14 + w + 8 * t) * 4096, SC1));
#pragma unroll
                    for (int k = 1; k < 4; ++k) a += __builtin_bit_cast(f32x4, LD16(rop, lane * 16 + gq * 1024, ((s * 4 + k) * 14 + w + 8 * t) * 4096, SC1));
                    const u32x2 xr = *(const u32x2*)(p.x + (size_t)(32 * s + tok) * DM + 32 * (w + 8 * t) + 8 * gq + 4 * hh);
                    const float xv[4] = {bf_lo(xr.x), bf_hi(xr.x), bf_lo(xr.y), bf_hi(xr.y)};
#pragma unroll
                    for (int e = 0; e < 4; ++e) { hv[t][4 * gq + e] = rbf(xv[e] + rbf(a[e])); ssum += hv[t][4 * gq + e]; }
                }
            }
        ssum = xsum32(ssum);
        float* red = (float*)(smem + LA_RED);
        if (lane < 32) red[w * 32 + tok] = ssum;
        BAR();
        float mean = 0.f;
#pragma unroll
        for (int k = 0; k < 8; ++k) mean += red[k * 32 + tok];
        mean *= (1.0f / DM);
        float s2 = 0.f;
#pragma unroll
        for (int t = 0; t < 2; ++t)
            if (t < nt) {
#pragma unroll
                for (int i = 0; i < 16; ++i) { const float d = hv[t][i] - mean; s2 += d * d; }
            }
        s2 = xsum32(s2);
        BAR();
        if (lane < 32) red[w * 32 + tok] = s2;
        BAR();
        float var = 0.f;
#pragma unroll
        for (int k = 0; k < 8; ++k) var += red[k * 32 + tok];
        var *= (1.0f / DM);
        const float mb = rbf(mean), den = rbf(sqrtf(rbf(rbf(var) + 1e-5f)));
#pragma unroll
        for (int t = 0; t < 2; ++t)
            if (t < nt) {
#pragma unroll
                for (int k = 0; k < 2; ++k) {
                    float y[8];
#pragma unroll
                    for (int e = 0; e < 8; ++e) y[e] = rbf(hv[t][8 * k + e] - mb) / den;
                    const int so = s * (int)SZ_SEGF + (2 * (w + 8 * t) + k) * 1024;
                    ST16(((u32x4){pk_bf(hv[t][8 * k], hv[t][8 * k + 1]), pk_bf(hv[t][8 * k + 2], hv[t][8 * k + 3]), pk_bf(hv[t][8 * k + 4], hv[t][8 * k + 5]), pk_bf(hv[t][8 * k + 6], hv[t][8 * k + 7])}), rhd, lane * 16, so, SC1);
                    ST16(((u32x4){pk_bf(y[0], y[1]), pk_bf(y[2], y[3]), pk_bf(y[4], y[5]), pk_bf(y[6], y[7])}), rhn, lane * 16, so, SC1);
                }
            }
        asm volatile("s_waitcnt vmcnt(0)" ::: "memory");
        BAR();
        if (tid_opaque() == 0) st_agent(ctrl + CT_H(s), 1u);
    }
    BAR();
}

constexpr int K2_LDS = GEMM_LDS + 256;

__global__ void __launch_bounds__(512, 2) k2_main(Params p) {
    extern __shared__ __attribute__((aligned(16))) char smem[];
    const int tid = threadIdx.x;
    unsigned* ctrl = (unsigned*)(p.ws + OFF_CTRL);
    volatile LAS unsigned* bc = (volatile LAS unsigned*)(smem + GEMM_LDS);
    if (tid == 0) {
        const unsigned xc = xcc_id();
        const unsigned r = add_agent(ctrl + CT_TICK(xc), 1u);
        unsigned role;
        if ((xc == 0u || xc == 2u) && r < 32u) role = (xc >> 1) * 32u + r;
        else role = N_REC + add_agent(ctrl + CT_WTICK, 1u);
        asm volatile("s_waitcnt vmcnt(0)" ::: "memory");
        add_agent(ctrl + CT_TOTAL, 1u);
        if (role >= N_WG) {
            unsigned sp = 0;
            while (ld_agent(ctrl + CT_TOTAL) < gridDim.x && ++sp < SPIN_CAP) __builtin_amdgcn_s_sleep(8);
            const unsigned c0 = min(ld_agent(ctrl + CT_TICK(0)), 32u), c2 = min(ld_agent(ctrl + CT_TICK(2)), 32u);
            const unsigned mi = role - N_WG;
            role = mi < 32u - c0 ? c0 + mi : 32u + c2 + (mi - (32u - c0));
        }
        bc[0] = role;
    }
    __syncthreads();
    const int role = __builtin_amdgcn_readfirstlane(bc[0]);
    __syncthreads();
    if (role < N_REC) { rec_role(p, smem, role); return; }
    if (role < N_REC + N_GEMM) { gemm448<0>(p, smem, role - N_REC, 0, 32); __syncthreads(); }
    else if (role < N_REC + N_GEMM + N_F1) { const int i = role - N_REC - N_GEMM; ffn1_role(p, smem, i >> 1, i & 1); return; }
    else if (role < N_REC + N_GEMM + N_F1 + N_F2) { const int i = role - N_REC - N_GEMM - N_F1; ffn2_role(p, smem, i >> 1, i & 1); return; }
    for (;;) {
        if (tid_opaque() == 0) bc[0] = add_agent(ctrl + CT_ATICK, 1u);
        __syncthreads();
        const int item = __builtin_amdgcn_readfirstlane(bc[0]);
        __syncthreads();
        if (item >= 4 * NSEG) break;
        if (tid_opaque() < 64) STAMP(256 + item, 0);
        attn_item(p, smem, item >> 2, item & 3);
        if (tid_opaque() < 64) STAMP(256 + item, 1);
    }
}

extern "C" void kernel_launch(void* const* d_in, const int* in_sizes, int n_in, void* d_out, int out_size, void* d_ws, size_t ws_size, hipStream_t stream) {
    Params p;
    p.x = (const uint16_t*)d_in[0]; p.mem0 = (const uint16_t*)d_in[1];
    p.wq = (const uint16_t*)d_in[2]; p.wk = (const uint16_t*)d_in[3]; p.wv = (const uint16_t*)d_in[4]; p.wo = (const uint16_t*)d_in[5];
    p.w1 = (const uint16_t*)d_in[8]; p.b1 = (const uint16_t*)d_in[9]; p.w2 = (const uint16_t*)d_in[10]; p.b2 = (const uint16_t*)d_in[11];
    p.out = (uint16_t*)d_out; p.ws = (char*)d_ws;
    uintptr_t hx = ((uintptr_t)d_ws + OFF_END + ((size_t)2 << 20) - 1) & ~(uintptr_t)(((size_t)2 << 20) - 1);
    p.hx = (char*)hx;
    (void)hipFuncSetAttribute((const void*)k1_prep, hipFuncAttributeMaxDynamicSharedMemorySize, K1_LDS);
    (void)hipFuncSetAttribute((const void*)k2_main, hipFuncAttributeMaxDynamicSharedMemorySize, K2_LDS);
    hipLaunchKernelGGL(k1_prep, dim3(K1_GRID), dim3(256), K1_LDS, stream, p);
    hipLaunchKernelGGL(k2_main, dim3(N_WG), dim3(512), K2_LDS, stream, p);
}
```

```cpp
#include <hip/hip_runtime.h>
#include <hip/hip_bf16.h>
#include <stdint.h>

typedef __bf16 bf16x8 __attribute__((ext_vector_type(8)));
typedef __bf16 bf16x2 __attribute__((ext_vector_type(2)));
typedef short s16x4 __attribute__((ext_vector_type(4)));
typedef float f32x2 __attribute__((ext_vector_type(2)));
typedef float f32x4 __attribute__((ext_vector_type(4)));
typedef float f32x16 __attribute__((ext_vector_type(16)));
typedef unsigned u32x2 __attribute__((ext_vector_type(2)));
typedef unsigned u32x4 __attribute__((ext_vector_type(4)));
#define LAS __attribute__((address_space(3)))
#define GAS __attribute__((address_space(1)))
#define DI __device__ __forceinline__
typedef __amdgpu_buffer_rsrc_t rsrc_t;

constexpr int DM = 448, NH = 16, DH = 256, SEQ = 2048, NSEG = 64, FF = 1792;

constexpr size_t OFF_CTRL = 0;
constexpr size_t SZ_CTRL = 262144;
constexpr size_t OFF_XNF = OFF_CTRL + SZ_CTRL;
constexpr size_t SZ_SEGF = 28 * 1024;
constexpr size_t OFF_WCT = OFF_XNF + 64 * SZ_SEGF;
constexpr size_t SZ_WCT = (size_t)448 * 56 * 512;
constexpr size_t OFF_KT = OFF_WCT + SZ_WCT;
constexpr size_t SZ_KT = (size_t)64 * 16 * 28672;
constexpr size_t OFF_VT = OFF_KT + SZ_KT;
constexpr size_t OFF_KM = OFF_VT + SZ_KT;
constexpr size_t SZ_KM = (size_t)64 * 16 * 7168;
constexpr size_t OFF_VM = OFF_KM + SZ_KM;
constexpr size_t OFF_OP = OFF_VM + SZ_KM;
constexpr size_t SZ_OP = (size_t)64 * 4 * 57344;
constexpr size_t OFF_HD = OFF_OP + SZ_OP;
constexpr size_t OFF_HNF = OFF_HD + 64 * SZ_SEGF;
constexpr size_t OFF_RF = OFF_HNF + 64 * SZ_SEGF;
constexpr size_t SZ_RF = (size_t)64 * 112 * 1024;
constexpr size_t OFF_END = OFF_RF + SZ_RF;
constexpr size_t SZ_HX = (size_t)4 << 20;

#define CT_TICK(x) (64 * (x))
#define CT_WTICK (64 * 16)
#define CT_TOTAL (64 * 17)
#define CT_ATICK (64 * 18)
#define CT_ABORT (64 * 19)
#define CT_PROBE(c) (57344 + (c) * 32)
#define CT_START(sh) (64 * (22 + (sh)))
#define CT_A(s) (64 * (32 + (s)))
#define CT_H(s) (49152 + (s) * 32)
#define CT_R(s) (49152 + 2048 + (s) * 32)
#define CT_KV(s, sh) (8192 + ((s) * 8 + (sh)) * 32)
#define CT_M(s, sh) (8192 + 16384 + ((s) * 8 + (sh)) * 32)
constexpr unsigned KV_PER_SHARD = 14, M_PER_SHARD = 8;

#define STAMP(wg, idx) do {} while (0)
#define RSTAMP(wv, idx) do {} while (0)
#define RVAL(wv, idx, val) do {} while (0)
constexpr int N_REC = 64, N_GEMM = 112, N_F1 = 28, N_F2 = 28, N_WG = 256;
constexpr unsigned SPIN_CAP = 1u << 15;
constexpr float LOG2E = 1.4426950408889634f;

struct Params {
    const uint16_t* x; const uint16_t* mem0; const uint16_t* wq; const uint16_t* wk; const uint16_t* wv; const uint16_t* wo;
    const uint16_t* w1; const uint16_t* b1; const uint16_t* w2; const uint16_t* b2;
    uint16_t* out; char* ws; char* hx;
};

DI float bf_lo(unsigned v) { return __builtin_bit_cast(float, v << 16); }
DI float bf_hi(unsigned v) { return __builtin_bit_cast(float, v & 0xffff0000u); }
DI unsigned pk_bf(float a, float b) { bf16x2 r = {(__bf16)a, (__bf16)b}; return __builtin_bit_cast(unsigned, r); }
DI float rbf(float a) { return bf_lo(pk_bf(a, a)); }
DI float dpp_ror8(float v) { return __builtin_bit_cast(float, __builtin_amdgcn_update_dpp(0, __builtin_bit_cast(int, v), 0x128, 0xf, 0xf, true)); }
DI unsigned fb(float v) { return __builtin_bit_cast(unsigned, v); }
DI float bfl(unsigned v) { return __builtin_bit_cast(float, v); }
DI float xsum32(float v) { auto r = __builtin_amdgcn_permlane32_swap(fb(v), fb(v), false, false); return bfl(r[0]) + bfl(r[1]); }
DI float xsum16(float v) { auto r = __builtin_amdgcn_permlane16_swap(fb(v), fb(v), false, false); return bfl(r[0]) + bfl(r[1]); }
DI float xmax32(float v) { auto r = __builtin_amdgcn_permlane32_swap(fb(v), fb(v), false, false); return fmaxf(bfl(r[0]), bfl(r[1])); }
DI float xmax16(float v) { auto r = __builtin_amdgcn_permlane16_swap(fb(v), fb(v), false, false); return fmaxf(bfl(r[0]), bfl(r[1])); }
DI f32x4 mfma16(u32x4 a, u32x4 b, f32x4 c) { return __builtin_amdgcn_mfma_f32_16x16x32_bf16(__builtin_bit_cast(bf16x8, a), __builtin_bit_cast(bf16x8, b), c, 0, 0, 0); }
DI f32x4 mfma16k16(u32x2 a, u32x2 b, f32x4 c) { return __builtin_amdgcn_mfma_f32_16x16x16bf16_1k(__builtin_bit_cast(s16x4, a), __builtin_bit_cast(s16x4, b), c, 0, 0, 0); }
DI f32x16 mfma32(u32x4 a, u32x4 b, f32x16 c) { return __builtin_amdgcn_mfma_f32_32x32x16_bf16(__builtin_bit_cast(bf16x8, a), __builtin_bit_cast(bf16x8, b), c, 0, 0, 0); }
DI rsrc_t mk_rsrc(const void* p, unsigned bytes) { return __builtin_amdgcn_make_buffer_rsrc((void*)p, 0, bytes, 0x00020000); }
#define LD16(r, vo, so, aux) __builtin_amdgcn_raw_buffer_load_b128((r), (vo), (so), (aux))
#define LD8(r, vo, so, aux) __builtin_amdgcn_raw_buffer_load_b64((r), (vo), (so), (aux))
#define ST16(v, r, vo, so, aux) do { const u32x4 st16_v_ = (v); __builtin_amdgcn_raw_buffer_store_b128(st16_v_, (r), (vo), (so), (aux)); asm volatile("s_nop 1" :: "v"(st16_v_)); } while (0)
#define ST8(v, r, vo, so, aux) __builtin_amdgcn_raw_buffer_store_b64((v), (r), (vo), (so), (aux))
#define ST4(v, r, vo, so, aux) __builtin_amdgcn_raw_buffer_store_b32((v), (r), (vo), (so), (aux))
constexpr int SC1 = 16;
#define BAR() do { asm volatile("s_waitcnt lgkmcnt(0)" ::: "memory"); __builtin_amdgcn_s_barrier(); asm volatile("" ::: "memory"); } while (0)
#define GLDS(src, dst, aux) __builtin_amdgcn_global_load_lds((const GAS void*)(src), (LAS void*)(dst), 16, 0, (aux))
DI unsigned ld_agent(const unsigned* p) { return __hip_atomic_load((const GAS unsigned*)p, __ATOMIC_RELAXED, __HIP_MEMORY_SCOPE_AGENT); }
DI void st_agent(unsigned* p, unsigned v) { __hip_atomic_store((GAS unsigned*)p, v, __ATOMIC_RELAXED, __HIP_MEMORY_SCOPE_AGENT); }
DI unsigned add_agent(unsigned* p, unsigned v) { return __hip_atomic_fetch_add((GAS unsigned*)p, v, __ATOMIC_RELAXED, __HIP_MEMORY_SCOPE_AGENT); }
DI int tid_opaque() { int t = threadIdx.x; asm volatile("" : "+v"(t)); return t; }
DI unsigned xcc_id() { return (unsigned)__builtin_amdgcn_s_getreg(20 | (3 << 11)) & 0xfu; }
DI float wave_sum(float v) {
    v += dpp_ror8(v);
    v += __builtin_bit_cast(float, __builtin_amdgcn_update_dpp(0, __builtin_bit_cast(int, v), 0x124, 0xf, 0xf, true));
    v += __builtin_bit_cast(float, __builtin_amdgcn_update_dpp(0, __builtin_bit_cast(int, v), 0x122, 0xf, 0xf, true));
    v += __builtin_bit_cast(float, __builtin_amdgcn_update_dpp(0, __builtin_bit_cast(int, v), 0x121, 0xf, 0xf, true));
    return xsum32(xsum16(v));
}
DI void wait_count(unsigned* ctrl, int word, unsigned target, int slp) {
    for (unsigned sp = 0; sp < SPIN_CAP; ++sp) {
        unsigned v = __builtin_amdgcn_readfirstlane(ld_agent(ctrl + word));
        if (v >= target) return;
        __builtin_amdgcn_s_sleep(2);
        for (int i = 0; i < slp; ++i) __builtin_amdgcn_s_sleep(8);
        if ((sp & 255u) == 255u && __builtin_amdgcn_readfirstlane(ld_agent(ctrl + CT_ABORT))) return;
    }
    st_agent(ctrl + CT_ABORT, 1u);
}

DI void wait_shards(unsigned* ctrl, int word, unsigned target, int slp) {
    const int lane = threadIdx.x & 7;
    for (unsigned sp = 0; sp < SPIN_CAP; ++sp) {
        if (!__any(ld_agent(ctrl + word + lane * 32) < target)) return;
        __builtin_amdgcn_s_sleep(2);
        for (int i = 0; i < slp; ++i) __builtin_amdgcn_s_sleep(8);
        if ((sp & 63u) == 63u && __builtin_amdgcn_readfirstlane(ld_agent(ctrl + CT_ABORT))) return;
    }
    st_agent(ctrl + CT_ABORT, 1u);
}

constexpr int K1_GRID = 512;
constexpr int K1_Q = 2 * 112 * 128;
constexpr int K1_LDS = 2 * K1_Q + 1024;

__global__ void __launch_bounds__(256, 2) k1_prep(Params p) {
    extern __shared__ __attribute__((aligned(16))) char smem[];
    const int b = blockIdx.x, tid = threadIdx.x, lane = tid & 63, w = __builtin_amdgcn_readfirstlane(tid >> 6);
    {
        const int m = (b & 7) + 8 * (b >> 7), tr = (b >> 5) & 3, tc = (b >> 3) & 3, hd = m & 15;
        const bool isA = m < 16;
        const uint16_t* arow = (isA ? p.wk : p.wv) + (size_t)(112 * tr) * 4096 + hd * 256;
        const uint16_t* brow = p.wq + (size_t)(112 * tc) * 4096 + hd * 256;
        const uint16_t* bcol = p.wo + (size_t)hd * 256 * 448 + 112 * tc;
        auto stage = [&](int kq, int slot) {
            char* base = smem + slot * K1_Q;
#pragma unroll
            for (int i = 0; i < 4; ++i) {
                const int j = w + 4 * i;
                if (j < 14) {
                    const int r = 8 * j + (lane >> 3), c = (lane & 7) ^ (r & 7);
                    GLDS(arow + (size_t)r * 4096 + kq * 64 + c * 8, base + j * 1024, 0);
                    if (isA) GLDS(brow + (size_t)r * 4096 + kq * 64 + c * 8, base + 14336 + j * 1024, 0);
                    else { const int ch = 64 * j + lane, e = ch / 14; GLDS(bcol + (size_t)(kq * 64 + e) * 448 + (ch - 14 * e) * 8, base + 14336 + j * 1024, 0); }
                }
            }
        };
        stage(0, 0); stage(1, 1);
        const int row = 4 * b + w;
        u32x4 raw = {0u, 0u, 0u, 0u};
        if (lane < 56) raw = *(const u32x4*)(p.x + (size_t)row * DM + lane * 8);
        if (tid < 32) *(u32x4*)(p.ws + OFF_CTRL + ((size_t)b * 32 + tid) * 16) = (u32x4){0u, 0u, 0u, 0u};
        {
            float v[8];
            v[0] = bf_lo(raw.x); v[1] = bf_hi(raw.x); v[2] = bf_lo(raw.y); v[3] = bf_hi(raw.y);
            v[4] = bf_lo(raw.z); v[5] = bf_hi(raw.z); v[6] = bf_lo(raw.w); v[7] = bf_hi(raw.w);
            float s = 0.f;
#pragma unroll
            for (int e = 0; e < 8; ++e) s += v[e];
            const float mean = wave_sum(s) * (1.0f / DM);
            float s2 = 0.f;
            if (lane < 56) {
#pragma unroll
                for (int e = 0; e < 8; ++e) { const float d = v[e] - mean; s2 += d * d; }
            }
            const float var = wave_sum(s2) * (1.0f / DM);
            const float mb = rbf(mean);
            const float den = rbf(sqrtf(rbf(rbf(var) + 1e-5f)));
            if (lane < 56) {
                float y[8];
#pragma unroll
                for (int e = 0; e < 8; ++e) y[e] = rbf(v[e] - mb) / den;
                const u32x4 o = {pk_bf(y[0], y[1]), pk_bf(y[2], y[3]), pk_bf(y[4], y[5]), pk_bf(y[6], y[7])};
                *(u32x4*)(p.ws + OFF_XNF + (size_t)(row >> 5) * SZ_SEGF + ((lane >> 1) * 64 + (lane & 1) * 32 + (row & 31)) * 16) = o;
            }
        }
        f32x4 acc[2][7];
#pragma unroll
        for (int i = 0; i < 2; ++i)
#pragma unroll
            for (int j = 0; j < 7; ++j) acc[i][j] = (f32x4){0.f, 0.f, 0.f, 0.f};
        const int x = lane & 15, q = lane >> 4;
        const int nst = w < 2 ? 8 : 6;
#pragma unroll
        for (int kq = 0; kq < 4; ++kq) {
            if (kq < 3) { if (nst == 8) asm volatile("s_waitcnt vmcnt(8)" ::: "memory"); else asm volatile("s_waitcnt vmcnt(6)" ::: "memory"); }
            else asm volatile("s_waitcnt vmcnt(0)" ::: "memory");
            BAR();
            const char* As = smem + (kq & 1) * K1_Q; const char* Bs = As + 14336;
#pragma unroll
            for (int ks = 0; ks < 2; ++ks) {
                u32x4 bf[7], af[2];
#pragma unroll
                for (int j = 0; j < 7; ++j) {
                    if (isA) bf[j] = *(const u32x4*)(Bs + (16 * j + x) * 128 + (((4 * ks + q) ^ (x & 7)) * 16));
                    else {
                        const int r0 = 32 * ks + 8 * q + (x >> 2), cb = (16 * j + 4 * (x & 3)) * 2;
                        const s16x4 lo = __builtin_amdgcn_ds_read_tr16_b64_v4i16((LAS s16x4*)(Bs + r0 * 224 + cb));
                        const s16x4 hi = __builtin_amdgcn_ds_read_tr16_b64_v4i16((LAS s16x4*)(Bs + (r0 + 4) * 224 + cb));
                        const u32x2 l2 = __builtin_bit_cast(u32x2, lo), h2 = __builtin_bit_cast(u32x2, hi);
                        bf[j] = (u32x4){l2.x, l2.y, h2.x, h2.y};
                    }
                }
#pragma unroll
                for (int i = 0; i < 2; ++i) { const int rt = w + 4 * i < 7 ? w + 4 * i : 6; af[i] = *(const u32x4*)(As + (16 * rt + x) * 128 + (((4 * ks + q) ^ (x & 7)) * 16)); }
#pragma unroll
                for (int i = 0; i < 2; ++i)
#pragma unroll
                    for (int j = 0; j < 7; ++j) acc[i][j] = mfma16(af[i], bf[j], acc[i][j]);
            }
            if (kq + 2 < 4) { BAR(); stage(kq + 2, kq & 1); }
        }
        const float sc = isA ? 0.0625f : 1.0f;
        char* wct = p.ws + OFF_WCT;
#pragma unroll
        for (int i = 0; i < 2; ++i) {
            const int rt = w + 4 * i;
            if (rt < 7) {
#pragma unroll
                for (int j = 0; j < 7; ++j) {
                    const int np = (isA ? 0 : 7168) + hd * 448 + 112 * tc + 16 * j + x;
                    const int k = 112 * tr + 16 * rt + 4 * q;
                    const u32x2 v = {pk_bf(acc[i][j][0] * sc, acc[i][j][1] * sc), pk_bf(acc[i][j][2] * sc, acc[i][j][3] * sc)};
                    *(u32x2*)(wct + ((size_t)((np >> 5) * 56 + (k >> 3)) * 32 + (np & 31)) * 16 + ((k >> 2) & 1) * 8) = v;
                }
            }
        }
    }
}

constexpr int GEMM_LDS = 2 * 2 * 28 * 1024;

template <int MODE>
DI void gemm448(const Params& p, char* smem, int slice, int seg0, int npair) {
    const int tid = threadIdx.x, lane = tid & 63, w = __builtin_amdgcn_readfirstlane(tid >> 6);
    const int ct = w & 3, th = w >> 2, T = 4 * slice + ct;
    unsigned* ctrl = (unsigned*)(p.ws + OFF_CTRL);
    const char* src = p.ws + (MODE == 0 ? OFF_XNF : OFF_HNF) + (size_t)seg0 * SZ_SEGF;
    u32x4 wf[28];
    {
        const rsrc_t rw = mk_rsrc(p.ws + OFF_WCT, (unsigned)SZ_WCT);
#pragma unroll
        for (int ks = 0; ks < 28; ++ks) wf[ks] = LD16(rw, lane * 16 + (ks & 3) * 1024, T * 28672 + (ks & ~3) * 1024, 0);
    }
#pragma unroll
    for (int i = 0; i < 7; ++i) GLDS(src + (w * 7 + i) * 1024 + lane * 16, smem + (w * 7 + i) * 1024, 0);
    asm volatile("s_waitcnt vmcnt(0)" ::: "memory");
#pragma unroll
    for (int i = 0; i < 7; ++i) GLDS(src + (size_t)57344 + (w * 7 + i) * 1024 + lane * 16, smem + 57344 + (w * 7 + i) * 1024, 0);
    if (MODE == 0 && w == 0) STAMP(64 + slice, 1);
    const bool isK = (MODE == 0) && (T < 224);
    const int Th = isK ? T : T - 224, hd = Th / 14, ctile = Th % 14;
    const rsrc_t ro = (MODE == 0) ? mk_rsrc(p.ws + (isK ? OFF_KT : OFF_VT), (unsigned)SZ_KT) : mk_rsrc(p.ws + OFF_RF, (unsigned)SZ_RF);
    u32x2 bias[4];
    if (MODE == 1) {
#pragma unroll
        for (int g = 0; g < 4; ++g) bias[g] = *(const u32x2*)(p.b1 + 32 * T + 8 * g + 4 * (lane >> 5));
    }
    for (int pr = 0; pr < npair; ++pr) {
        if (pr >= 1) {
            if (pr + 1 < npair) asm volatile("s_waitcnt vmcnt(7)" ::: "memory"); else asm volatile("s_waitcnt vmcnt(0)" ::: "memory");
        }
        BAR();
        if (MODE == 0 && w == 0) STAMP(64 + slice, 2 + pr);
        if (MODE == 0 && pr >= 2 && tid_opaque() < 2) add_agent(ctrl + CT_KV(seg0 + 2 * (pr - 1) + tid, slice & 7), 1u);
        const char* xs = smem + (pr & 1) * 57344 + th * 28672 + lane * 16;
        f32x16 acc;
#pragma unroll
        for (int i = 0; i < 16; ++i) acc[i] = 0.f;
        if (MODE == 0 && !isK) {
#pragma unroll
            for (int ks = 0; ks < 28; ++ks) acc = mfma32(*(const u32x4*)(xs + ks * 1024), wf[ks], acc);
        } else {
#pragma unroll
            for (int ks = 0; ks < 28; ++ks) acc = mfma32(wf[ks], *(const u32x4*)(xs + ks * 1024), acc);
        }
        const int s = seg0 + 2 * pr + th;
        if (MODE == 0) {
            unsigned pk[8];
#pragma unroll
            for (int i = 0; i < 8; ++i) pk[i] = pk_bf(acc[2 * i], acc[2 * i + 1]);
            if (isK) {
#pragma unroll
                for (int k = 0; k < 2; ++k) {
                    auto r0 = __builtin_amdgcn_permlane32_swap(pk[4 * k], pk[4 * k + 2], false, false);
                    auto r1 = __builtin_amdgcn_permlane32_swap(pk[4 * k + 1], pk[4 * k + 3], false, false);
                    u32x4 v = {r0[0], r1[0], r0[1], r1[1]};
                    ST16(v, ro, lane * 16 + k * 1024, ((s * 16 + hd) * 56 + 4 * ctile) * 512, SC1);
                }
            } else {
#pragma unroll
                for (int k = 0; k < 2; ++k) {
                    u32x4 v = {pk[4 * k], pk[4 * k + 1], pk[4 * k + 2], pk[4 * k + 3]};
                    ST16(v, ro, lane * 16 + k * 1024, ((s * 16 + hd) * 14 + ctile) * 2048, SC1);
                }
            }
        } else {
            unsigned pk[8];
#pragma unroll
            for (int g = 0; g < 4; ++g) {
                float bv[4] = {bf_lo(bias[g].x), bf_hi(bias[g].x), bf_lo(bias[g].y), bf_hi(bias[g].y)};
                float r[4];
#pragma unroll
                for (int e = 0; e < 4; ++e) { float t = rbf(rbf(acc[4 * g + e]) + bv[e]); t = t > 0.f ? t : 0.f; r[e] = t * t; }
                pk[2 * g] = pk_bf(r[0], r[1]); pk[2 * g + 1] = pk_bf(r[2], r[3]);
            }
#pragma unroll
            for (int k = 0; k < 2; ++k) {
                u32x4 v = {pk[4 * k], pk[4 * k + 1], pk[4 * k + 2], pk[4 * k + 3]};
                ST16(v, ro, lane * 16 + k * 1024, (s * 112 + 2 * T) * 1024, 0);
            }
        }
        if (MODE == 0 && pr == 0) {
            asm volatile("s_waitcnt vmcnt(0)" ::: "memory");
            BAR();
            if (tid_opaque() < 2) add_agent(ctrl + CT_KV(seg0 + tid, slice & 7), 1u);
        }
        if (pr + 2 < npair) {
            BAR();
#pragma unroll
            for (int i = 0; i < 7; ++i) GLDS(src + (size_t)(pr + 2) * 57344 + (w * 7 + i) * 1024 + lane * 16, smem + (pr & 1) * 57344 + (w * 7 + i) * 1024, 0);
        }
    }
    if (MODE == 0) {
        asm volatile("s_waitcnt vmcnt(0)" ::: "memory");
        BAR();
        if (tid_opaque() < 2) add_agent(ctrl + CT_KV(seg0 + 2 * (npair - 1) + tid, slice & 7), 1u);
    }
}

DI void ffn1_role(const Params& p, char* smem, int slice, int par) {
    const int tid = threadIdx.x, lane = tid & 63, w = __builtin_amdgcn_readfirstlane(tid >> 6);
    const int ct = w & 3, kh = w >> 2, T = 4 * slice + ct, hh = lane >> 5;
    unsigned* ctrl = (unsigned*)(p.ws + OFF_CTRL);
    u32x4 wf[14];
    {
#pragma unroll
        for (int i = 0; i < 14; ++i) {
            const int ch = tid + 512 * i;
            *(u32x4*)(smem + (ch >> 4) * 272 + (ch & 15) * 16) = *(const u32x4*)(p.w1 + (size_t)(ch >> 4) * FF + 128 * slice + (ch & 15) * 8);
        }
        __syncthreads();
        const int blk = (lane >> 4) & 1, a = (lane & 15) >> 2, pp = lane & 3;
#pragma unroll
        for (int ks = 0; ks < 14; ++ks) {
            const int r0 = 16 * (14 * kh + ks) + 4 * hh + a, cb = (32 * ct + 16 * blk + 4 * pp) * 2;
            const s16x4 lo = __builtin_amdgcn_ds_read_tr16_b64_v4i16((LAS s16x4*)(smem + r0 * 272 + cb));
            const s16x4 hi = __builtin_amdgcn_ds_read_tr16_b64_v4i16((LAS s16x4*)(smem + (r0 + 8) * 272 + cb));
            const u32x2 l2 = __builtin_bit_cast(u32x2, lo), h2 = __builtin_bit_cast(u32x2, hi);
            wf[ks] = (u32x4){l2.x, l2.y, h2.x, h2.y};
        }
        __syncthreads();
    }
    u32x2 bias[4];
#pragma unroll
    for (int g = 0; g < 4; ++g) bias[g] = *(const u32x2*)(p.b1 + 32 * T + 8 * g + 4 * hh);
    const rsrc_t ro = mk_rsrc(p.ws + OFF_RF, (unsigned)SZ_RF);
    char* red = smem + 28672;
    for (int s = par; s < NSEG; s += 2) {
        if (w == 0) wait_count(ctrl, CT_H(s), 4u, s >= 60 ? 0 : 4);
        BAR();
        if (w == 0 && slice == 0 && s >= 62) STAMP(300 + s, 2);
        {
            const char* src = p.ws + OFF_HNF + (size_t)s * SZ_SEGF;
#pragma unroll
            for (int i = 0; i < 4; ++i) { const int k = w + 8 * i; if (k < 28) GLDS(src + k * 1024 + lane * 16, smem + k * 1024, SC1); }
        }
        asm volatile("s_waitcnt vmcnt(0)" ::: "memory");
        BAR();
        f32x16 acc;
#pragma unroll
        for (int i = 0; i < 16; ++i) acc[i] = 0.f;
        u32x4 xf[14];
#pragma unroll
        for (int ks = 0; ks < 14; ++ks) xf[ks] = *(const u32x4*)(smem + (14 * kh + ks) * 1024 + lane * 16);
        __builtin_amdgcn_sched_barrier(0);
#pragma unroll
        for (int ks = 0; ks < 14; ++ks) acc = mfma32(wf[ks], xf[ks], acc);
        if (kh == 1) {
#pragma unroll
            for (int g = 0; g < 4; ++g) *(f32x4*)(red + ((ct * 4 + g) * 64 + lane) * 16) = (f32x4){acc[4 * g], acc[4 * g + 1], acc[4 * g + 2], acc[4 * g + 3]};
        }
        BAR();
        if (kh == 0) {
            unsigned pk[8];
#pragma unroll
            for (int g = 0; g < 4; ++g) {
                const f32x4 o2 = *(const f32x4*)(red + ((ct * 4 + g) * 64 + lane) * 16);
                const float bv[4] = {bf_lo(bias[g].x), bf_hi(bias[g].x), bf_lo(bias[g].y), bf_hi(bias[g].y)};
                float r[4];
#pragma unroll
                for (int e = 0; e < 4; ++e) { float t = rbf(rbf(acc[4 * g + e] + o2[e]) + bv[e]); t = t > 0.f ? t : 0.f; r[e] = t * t; }
                pk[2 * g] = pk_bf(r[0], r[1]); pk[2 * g + 1] = pk_bf(r[2], r[3]);
            }
#pragma unroll
            for (int k = 0; k < 2; ++k) ST16(((u32x4){pk[4 * k], pk[4 * k + 1], pk[4 * k + 2], pk[4 * k + 3]}), ro, lane * 16 + k * 1024, (s * 112 + 2 * T) * 1024, SC1);
            asm volatile("s_waitcnt vmcnt(0)" ::: "memory");
        }
        BAR();
        if (w == 0 && slice == 0 && s >= 62) STAMP(300 + s, 3);
        if (tid_opaque() == 0) add_agent(ctrl + CT_R(s), 1u);
    }
}
DI void ffn2_role(const Params& p, char* smem, int ctile, int par) {
    const int tid = threadIdx.x, lane = tid & 63, w = __builtin_amdgcn_readfirstlane(tid >> 6);
    const int tok = lane & 31, hh = lane >> 5;
    unsigned* ctrl = (unsigned*)(p.ws + OFF_CTRL);
    u32x4 wf[14];
    {
#pragma unroll
        for (int i = 0; i < 14; ++i) {
            const int ch = tid + 512 * i;
            *(u32x4*)(smem + ch * 16) = *(const u32x4*)(p.w2 + (size_t)(ch >> 2) * DM + 32 * ctile + (ch & 3) * 8);
        }
        __syncthreads();
        const int blk = (lane >> 4) & 1, a = (lane & 15) >> 2, pp = lane & 3;
#pragma unroll
        for (int ks = 0; ks < 14; ++ks) {
            const int r0 = 16 * (14 * w + ks) + 4 * hh + a, cb = (16 * blk + 4 * pp) * 2;
            const s16x4 lo = __builtin_amdgcn_ds_read_tr16_b64_v4i16((LAS s16x4*)(smem + r0 * 64 + cb));
            const s16x4 hi = __builtin_amdgcn_ds_read_tr16_b64_v4i16((LAS s16x4*)(smem + (r0 + 8) * 64 + cb));
            const u32x2 l2 = __builtin_bit_cast(u32x2, lo), h2 = __builtin_bit_cast(u32x2, hi);
            wf[ks] = (u32x4){l2.x, l2.y, h2.x, h2.y};
        }
        __syncthreads();
    }
    const u32x2 br = *(const u32x2*)(p.b2 + 32 * ctile + 8 * (w & 3) + 4 * hh);
    const rsrc_t rr = mk_rsrc(p.ws + OFF_RF, (unsigned)SZ_RF), rh = mk_rsrc(p.ws + OFF_HD, (unsigned)(64 * SZ_SEGF));
    for (int s = par; s < NSEG; s += 2) {
        if (w == 0) wait_count(ctrl, CT_R(s), 14u, s >= 60 ? 0 : 4);
        BAR();
        if (w == 0 && ctile == 0 && s >= 62) STAMP(300 + s, 4);
        u32x4 xf[14];
#pragma unroll
        for (int ks = 0; ks < 14; ++ks) xf[ks] = LD16(rr, lane * 16, (s * 112 + 14 * w + ks) * 1024, SC1);
        u32x2 hr = {0u, 0u};
        if (w < 4) hr = LD8(rh, lane * 16 + (w & 1) * 8, s * (int)SZ_SEGF + (2 * ctile + (w >> 1)) * 1024, SC1);
        f32x16 acc;
#pragma unroll
        for (int i = 0; i < 16; ++i) acc[i] = 0.f;
#pragma unroll
        for (int ks = 0; ks < 14; ++ks) acc = mfma32(wf[ks], xf[ks], acc);
#pragma unroll
        for (int g = 0; g < 4; ++g) *(f32x4*)(smem + ((g * 8 + w) * 64 + lane) * 16) = (f32x4){acc[4 * g], acc[4 * g + 1], acc[4 * g + 2], acc[4 * g + 3]};
        BAR();
        if (w < 4) {
            f32x4 t[8];
#pragma unroll
            for (int k = 0; k < 8; ++k) t[k] = *(const f32x4*)(smem + ((w * 8 + k) * 64 + lane) * 16);
            const f32x4 o = ((t[0] + t[1]) + (t[2] + t[3])) + ((t[4] + t[5]) + (t[6] + t[7]));
            const float bv[4] = {bf_lo(br.x), bf_hi(br.x), bf_lo(br.y), bf_hi(br.y)};
            const float hv[4] = {bf_lo(hr.x), bf_hi(hr.x), bf_lo(hr.y), bf_hi(hr.y)};
            float y[4];
#pragma unroll
            for (int e = 0; e < 4; ++e) y[e] = hv[e] + rbf(rbf(o[e]) + bv[e]);
            *(u32x2*)(p.out + (size_t)(32 * s + tok) * DM + 32 * ctile + 8 * w + 4 * hh) = (u32x2){pk_bf(y[0], y[1]), pk_bf(y[2], y[3])};
            if (w == 0 && ctile == 0 && s >= 62) STAMP(300 + s, 5);
        }
    }
}

constexpr int L_F = 0;
constexpr int L_R = 14336;
constexpr int L_MX = L_R + 1792;
constexpr int L_R2 = L_MX + 128;

constexpr unsigned POLL_CAP = 1u << 14;
#define NOCOMM 0
DI u32x4 poll16(rsrc_t r, int vo, int so, unsigned* ctrl, unsigned& cap, unsigned code = 1u, unsigned* np = nullptr) {
    u32x4 v;
    unsigned sp = 0;
    bool bad;
    do {
        asm volatile("" ::: "memory");
        v = LD16(r, vo, so, SC1);
        bad = __any(max(max(v.x, v.y), max(v.z, v.w)) == 0xffffffffu);
    } while (bad && ++sp < cap);
    if (np) *np = sp;
    if (bad) { st_agent(ctrl + CT_ABORT, 1u); cap = 1u; }
    return v;
}
DI float fmax_raw(float a, float b) { float r; asm("v_max_f32 %0, %1, %2" : "=v"(r) : "v"(a), "v"(b)); return r; }
DI float fmax3_raw(float a, float b, float c) { float r; asm("v_max3_f32 %0, %1, %2, %3" : "=v"(r) : "v"(a), "v"(b), "v"(c)); return r; }

#define DELAY_X 7
#define DELAY_Y 0
#define K_Y 132
#define K_X 232
#define T_START 30
#define D_Y 25
#define D_X 25
DI unsigned later(unsigned a, unsigned b) { return (int)(a - b) >= 0 ? a : b; }
DI unsigned now_ticks() { return (unsigned)__builtin_amdgcn_s_memrealtime(); }
DI void wait_until(unsigned target) {
    unsigned long long t;
    unsigned d;
    asm volatile("s_memrealtime %0\n\ts_waitcnt lgkmcnt(0)" : "=s"(t) :: "memory");
    d = target - (unsigned)t;
    if (d - 1u < 4000u) {
        do {
            asm volatile("s_memrealtime %0\n\ts_waitcnt lgkmcnt(0)" : "=s"(t) :: "memory");
        } while ((int)(target - (unsigned)t) > 0);
    }
}
constexpr int L_TX = L_R2 + 4096;

template <bool BROLE, bool W7>
DI void rec_wave(const Params& p, char* smem, const int c, const int w) {
    const int tid = threadIdx.x, lane = tid & 63;
    const int x = lane & 15, q = lane >> 4, ap = (lane >> 3) & 1, i8 = lane & 7;
    const int h = c >> 2, c4 = c & 3;
    const int u = c >= 28 ? 1 : 0, J = c - 28 * u, g = 8 * u + w;
    unsigned* ctrl = (unsigned*)(p.ws + OFF_CTRL);
    const rsrc_t rhx = mk_rsrc(p.hx, (unsigned)SZ_HX), rkt = mk_rsrc(p.ws + OFF_KT, (unsigned)SZ_KT), rvt = mk_rsrc(p.ws + OFF_VT, (unsigned)SZ_KT);
    const rsrc_t rkm = mk_rsrc(p.ws + OFF_KM, (unsigned)SZ_KM), rvm = mk_rsrc(p.ws + OFF_VM, (unsigned)SZ_KM);
    unsigned cap = POLL_CAP;

    u32x4 wA[14], wB[14];
    auto load_w = [&](u32x4 (&wt)[14], int np) {
        const rsrc_t rw = mk_rsrc(p.ws + OFF_WCT, (unsigned)SZ_WCT);
        const int vo = ((np >> 5) * 56 * 32 + q * 32 + (np & 31)) * 16;
#pragma unroll
        for (int ks = 0; ks < 14; ++ks) wt[ks] = LD16(rw, vo + (ks & 1) * 2048, (ks & ~1) * 2048, 0);
    };
    if (W7 || w != 0) {
        if (!W7) load_w(wA, h * 448 + 112 * c4 + 16 * w + x);
        if (BROLE) load_w(wB, 7168 + g * 448 + 16 * J + x);
    }
    if (!W7 && w == 0) {
        unsigned best0 = ~0u, best1 = ~0u;
        for (int rep = 0; rep < 4; ++rep) {
            const int off = c * 65536 + 16384 + (rep & 1) * 8192 + 8064;
            const unsigned long long t0 = __builtin_amdgcn_s_memtime();
            ST4((unsigned)rep, rhx, 0, off, SC1);
            asm volatile("s_waitcnt vmcnt(0)" ::: "memory");
            unsigned v = __builtin_amdgcn_raw_buffer_load_b32(rhx, 0, off, SC1);
            asm volatile("s_waitcnt vmcnt(0)" : "+v"(v) :: "memory");
            const unsigned dt = (unsigned)(__builtin_amdgcn_s_memtime() - t0);
            if (rep & 1) best1 = min(best1, dt); else best0 = min(best0, dt);
        }
        if (lane == 0) st_agent(ctrl + CT_PROBE(c), 2u | (best1 < best0 ? 1u : 0u));
        load_w(wA, h * 448 + 112 * c4 + 16 * w + x);
        if (BROLE) load_w(wB, 7168 + g * 448 + 16 * J + x);
        unsigned long long pr = 0;
        for (unsigned sp = 0; sp < SPIN_CAP; ++sp) {
            const unsigned v = ld_agent(ctrl + CT_PROBE(lane));
            pr = __ballot((v & 1u) != 0u);
            if (!__any(v == 0u)) break;
            __builtin_amdgcn_s_sleep(4);
        }
        const int cnt = __builtin_popcountll((pr >> (lane & ~7)) & 0xffull);
        pr = __ballot(cnt > 4 || (cnt == 4 && ((pr >> lane) & 1ull)));
        if (lane == 0) { *(LAS unsigned long long*)(smem + L_TX + 8) = pr; }
    }
    if (!W7) {
        const int o = tid >> 3, i = tid & 7;
        *(u32x4*)(smem + L_F + (o * 16 + i) * 16) = *(const u32x4*)(p.mem0 + i * DM + o * 8);
        *(u32x4*)(smem + L_F + (o * 16 + 8 + i) * 16) = (u32x4){0u, 0u, 0u, 0u};
    }
    BAR();
    unsigned long long probe;
    {
        const u32x2 pv = *(LAS u32x2*)(smem + L_TX + 8);
        probe = ((unsigned long long)(unsigned)__builtin_amdgcn_readfirstlane(pv.y) << 32) | (unsigned)__builtin_amdgcn_readfirstlane(pv.x);
    }
    {
        const u32x4 ones = {~0u, ~0u, ~0u, ~0u};
        const int so = c * 65536 + (int)(((probe >> c) & 1ull) << 13);
#pragma unroll
        for (int i = 0; i < 4; ++i) ST16(ones, rhx, i * 16384 + tid * 16, so, SC1);
        asm volatile("s_waitcnt vmcnt(0)" ::: "memory");
        BAR();
        if (!W7 && w == 0) {
            if (lane == 0) add_agent(ctrl + CT_START(c & 7), 1u);
            wait_shards(ctrl, CT_KV(0, 0), KV_PER_SHARD, 0);
            wait_shards(ctrl, CT_KV(1, 0), KV_PER_SHARD, 0);
            for (unsigned sp = 0; sp < SPIN_CAP; ++sp) {
                if (!__any(ld_agent(ctrl + CT_START(lane & 7)) < 8u)) break;
                __builtin_amdgcn_s_sleep(4);
            }
        }
        BAR();
    }
    STAMP(c, w);
    auto phys = [](int lo) { return ((lo >> 13) << 14) + (lo & 8191); };
    const int vo_x0 = phys(tid * 16), vo_x1 = phys(7168 + tid * 16);
    const int lo_f = L_F + ((tid >> 3) * 16 + (tid & 7)) * 16;
    const int vo_yl = phys(16384 + (g * 4 + 2 * ap + (q >> 1)) * 256 + i8 * 32 + (q & 1) * 16);
    const int vo_ys = phys(16384 + c * 256 + lane * 4);
    const int vo_xs = phys(u * 7168 + J * 256 + lane * 4);
    const int vo_ts = phys(14336 + c * 16), vo_tl = phys(14336 + lane * 16);
    const int vo_k = (q * 32 + 8 * c4 + i8) * 16;
    const int vo_v = ((q & 1) * 32 + 16 * (J & 1) + x) * 16 + 8 * (q >> 1);
    const int vo_km = ((14 * c4 + 2 * w + (q >> 1)) * 8 + i8) * 16 + (q & 1) * 8;
    const int vo_vm = (16 * J + x) * 16 + (q & 1) * 8;
    const int scol = 112 * c4 + 16 * w + 4 * q;
    const int lo_s = L_F + ((scol >> 3) * 16 + x) * 16 + (scol & 4) * 2;
    auto step_off = [&](int s) { return s * 65536 + (int)(((probe >> s) & 1ull) << 13); };

    u32x2 v1 = {0u, 0u}, v2 = {0u, 0u}, vn1 = {0u, 0u}, vn2 = {0u, 0u};
    auto v_load = [&](int s) {
        const int so = ((s * 16 + g) * 14 + (J >> 1)) * 2048;
        vn1 = LD8(rvt, vo_v, so, SC1); vn2 = LD8(rvt, vo_v + 1024, so, SC1);
    };
    auto k_load = [&](int s) {
#pragma unroll
        for (int ks = 0; ks < 14; ++ks) wA[ks] = LD16(rkt, vo_k, (s * 16 + h) * 28672 + ks * 2048, SC1);
    };
    if (BROLE) v_load(0);
    if (W7) k_load(0);
    u32x4 fr[14];
    int kv_ready = 2;
    const unsigned long long m_ap = __ballot(ap != 0);
    const unsigned hm = (q >> 1) ? 0xffff0000u : 0x0000ffffu, m_a = ap ? 0u : hm, m_b = ap ? hm : 0u;
    const unsigned HI = 0xffff0000u;
    const float L2E = LOG2E, NL2E = -LOG2E;
#define SB() __builtin_amdgcn_sched_barrier(0)
    auto gather_t = [&](int s, int so_s, int n) {
        unsigned t, sp = 0;
        bool bad;
        do {
            asm volatile("" ::: "memory");
            t = __builtin_amdgcn_raw_buffer_load_b32(rhx, n > 1 ? vo_tl : phys(14336), so_s, SC1);
            bad = __any(lane < n && t == 0xffffffffu);
        } while (bad && ++sp < cap);
        if (bad) { st_agent(ctrl + CT_ABORT, 1u); cap = 1u; }
        const unsigned ref = __builtin_amdgcn_readfirstlane(t);
        int d = lane < n ? (int)(t - ref) : -0x7fffffff;
        d = max(d, __builtin_amdgcn_update_dpp(d, d, 0x128, 0xf, 0xf, false));
        d = max(d, __builtin_amdgcn_update_dpp(d, d, 0x124, 0xf, 0xf, false));
        d = max(d, __builtin_amdgcn_update_dpp(d, d, 0x122, 0xf, 0xf, false));
        d = max(d, __builtin_amdgcn_update_dpp(d, d, 0x121, 0xf, 0xf, false));
        { auto r = __builtin_amdgcn_permlane16_swap((unsigned)d, (unsigned)d, false, false); d = max((int)r[0], (int)r[1]); }
        { auto r = __builtin_amdgcn_permlane32_swap((unsigned)d, (unsigned)d, false, false); d = max((int)r[0], (int)r[1]); }
        if (lane == 0) *(LAS unsigned*)(smem + L_TX + (s & 1) * 4) = ref + (unsigned)d;
    };
    auto publish_t = [&](int so_s) { if (lane == 0) ST4(min(now_ticks(), 0xfffffffeu), rhx, vo_ts, so_s, SC1); };
    unsigned x_tries = 0;
    auto gather_x = [&](int so_s) {
        auto pass = [&]() {
            asm volatile("" ::: "memory");
            const u32x4 a0 = LD16(rhx, vo_x0, so_s, SC1), a1 = LD16(rhx, vo_x1, so_s, SC1);
            *(u32x4*)(smem + lo_f) = a0;
            *(u32x4*)(smem + lo_f + 128) = a1;
            return NOCOMM == 0 && __any(max(max(max(a0.x, a0.y), max(a0.z, a0.w)), max(max(a1.x, a1.y), max(a1.z, a1.w))) == 0xffffffffu);
        };
        x_tries = 0;
        if (__builtin_expect(pass(), 0)) {
            bool bad = true;
            while (bad && ++x_tries < cap) bad = pass();
            if (bad) { st_agent(ctrl + CT_ABORT, 1u); cap = 1u; }
        }
    };
    auto a_mfma = [&]() {
#pragma unroll
        for (int ks = 0; ks < 4; ++ks) fr[ks] = *(const u32x4*)(smem + L_F + ks * 1024 + lane * 16);
        SB();
        f32x4 acc;
        asm volatile("v_mfma_f32_16x16x32_bf16 %0, %1, %2, 0" : "=&v"(acc) : "v"(wA[0]), "v"(fr[0]));
        fr[4] = *(const u32x4*)(smem + L_F + 4 * 1024 + lane * 16);
        SB();
#pragma unroll
        for (int ks = 1; ks < 14; ++ks) {
            asm volatile("v_mfma_f32_16x16x32_bf16 %0, %1, %2, %0" : "+v"(acc) : "v"(wA[ks]), "v"(fr[ks]));
            if (ks + 4 < 14) fr[ks + 4] = *(const u32x4*)(smem + L_F + (ks + 4) * 1024 + lane * 16);
            SB();
        }
        float a0, a1, a2, a3;
        asm volatile("s_nop 7\n\t"
                     "v_add_f32_dpp %0, %4, %4 row_ror:8 row_mask:0xf bank_mask:0xf\n\tv_add_f32_dpp %1, %5, %5 row_ror:8 row_mask:0xf bank_mask:0xf\n\t"
                     "v_add_f32_dpp %2, %6, %6 row_ror:8 row_mask:0xf bank_mask:0xf\n\tv_add_f32_dpp %3, %7, %7 row_ror:8 row_mask:0xf bank_mask:0xf"
                     : "=&v"(a0), "=&v"(a1), "=&v"(a2), "=&v"(a3) : "v"(acc[0]), "v"(acc[1]), "v"(acc[2]), "v"(acc[3]));
        return (u32x2){pk_bf(a0, a1), pk_bf(a2, a3)};
    };
    auto u_part = [&]() {
        f32x4 ua = {0.f, 0.f, 0.f, 0.f};
#pragma unroll
        for (int ks = 0; ks < 14; ++ks) ua = mfma16(fr[ks], wB[ks], ua);
#pragma unroll
        for (int r = 0; r < 4; ++r) ua[r] = xsum32(ua[r]);
        return (u32x2){pk_bf(ua[0], ua[1]), pk_bf(ua[2], ua[3])};
    };

    int so_s = step_off(0);
    if (W7) {
        if (c == 0 && lane == 0) ST4(min(now_ticks() - T_START, 0xfffffffeu), rhx, vo_ts, so_s, SC1);
        gather_t(0, so_s, 1);
    }
    for (int s = 0; s < NSEG - 1; ++s) {
        u32x2 tb;
        if (BROLE) { v1 = vn1; v2 = vn2; }
        BAR();
        RSTAMP(1, s * 16 + 0);
        if (BROLE) v_load(s + 1);
        if (!W7) {
            const u32x2 sop = *(const u32x2*)(smem + lo_s);
            tb = a_mfma();
            f32x4 st;
            float s0, s1, s2, s3;
            asm volatile("s_nop 1\n\tv_mfma_f32_16x16x16_bf16 %0, %1, %2, 0" : "=&v"(st) : "v"(tb), "v"(sop));
            asm volatile("s_nop 7\n\t"
                         "v_add_f32_dpp %0, %4, %4 row_ror:8 row_mask:0xf bank_mask:0xf\n\tv_add_f32_dpp %1, %5, %5 row_ror:8 row_mask:0xf bank_mask:0xf\n\t"
                         "v_add_f32_dpp %2, %6, %6 row_ror:8 row_mask:0xf bank_mask:0xf\n\tv_add_f32_dpp %3, %7, %7 row_ror:8 row_mask:0xf bank_mask:0xf"
                         : "=&v"(s0), "=&v"(s1), "=&v"(s2), "=&v"(s3) : "v"(st[0]), "v"(st[1]), "v"(st[2]), "v"(st[3]));
            RSTAMP(1, s * 16 + 1);
            if (x < 8 && q < 2) *(f32x4*)(smem + L_R + (w * 16 + 2 * x + q) * 16) = (f32x4){s0, s1, s2, s3};
        } else {
            tb = a_mfma();
            if (x < 8 && q < 2) *(u32x2*)(smem + L_MX + (2 * x + q) * 8) = tb;
        }
        BAR();
        RSTAMP(1, s * 16 + 2);
        if (!W7 && w == 0) {
            const int chunk = lane >> 2, part = lane & 3;
            f32x2 t[7];
#pragma unroll
            for (int i = 0; i < 7; ++i) t[i] = *(const f32x2*)(smem + L_R + (i * 16 + chunk) * 16 + (part & 1) * 8);
            unsigned mxv = *(const unsigned*)(smem + L_MX + chunk * 8 + (part & 1) * 4);
            asm volatile("" : "+v"(mxv));
            const f32x2 sum = ((t[0] + t[1]) + (t[2] + t[3])) + ((t[4] + t[5]) + t[6]);
            unsigned mmv = pk_bf(sum[0], sum[1]);
            asm volatile("" : "+v"(mmv));
            ST4(min(part < 2 ? mxv : mmv, 0xfffffffeu), rhx, vo_ys, so_s, SC1);
            RSTAMP(0, s * 16 + 3); RVAL(0, s * 16 + 14, (unsigned)__builtin_amdgcn_s_memrealtime());
        }
        const unsigned t_b2 = now_ticks();
        SB();
        const unsigned t_prev = __builtin_amdgcn_readfirstlane(*(LAS unsigned*)(smem + L_TX + (s & 1) * 4));
        const int so_n = step_off(s + 1);
        if (!W7 && s > 0 && w == 3 && tid_opaque() == 192) add_agent(ctrl + CT_M(s - 1, c & 7), 1u);
        if (!W7 && x < 8) ST8(tb, rkm, vo_km, (s * 16 + h) * 7168, SC1);
        if (W7) k_load(s + 1);
        unsigned t_x = t_prev + K_X;
        if (BROLE) {
            RSTAMP(1, s * 16 + 4);
            wait_until(later(t_prev + K_Y, t_b2 + D_Y));
            RSTAMP(1, s * 16 + 5);
            asm volatile("" ::: "memory");
            u32x4 y = LD16(rhx, vo_yl, so_s, SC1);
            SB();
            const u32x2 ub = u_part();
            SB();
            unsigned sp = 0;
            float o0, o1_, o2_, o3;
            auto pass = [&]() {
                unsigned long long bad;
                float x0, x1, x2, x3, sm, p0, p1, p2, p3, m, t;
                asm volatile(
                    "v_lshlrev_b32 %[p0], 16, %[y2]\n\tv_and_b32 %[p1], %[hi], %[y2]\n\tv_lshlrev_b32 %[p2], 16, %[y3]\n\tv_and_b32 %[p3], %[hi], %[y3]\n\t"
                    "v_lshlrev_b32 %[x0], 16, %[y0]\n\tv_and_b32 %[x1], %[hi], %[y0]\n\t"
                    "v_permlane32_swap_b32 %[p0], %[p1]\n\tv_permlane32_swap_b32 %[p2], %[p3]\n\t"
                    "v_lshlrev_b32 %[x2], 16, %[y1]\n\tv_and_b32 %[x3], %[hi], %[y1]\n\t"
                    "v_add_f32 %[p0], %[p0], %[p1]\n\tv_add_f32 %[p2], %[p2], %[p3]\n\t"
                    "v_max3_u32 %[t], %[y0], %[y1], %[y2]\n\t"
                    "v_cndmask_b32 %[p1], %[p2], %[p0], %[ap]\n\tv_cndmask_b32 %[p3], %[p0], %[p2], %[ap]\n\t"
                    "v_max3_f32 %[m], %[x0], %[x1], %[x2]\n\t"
                    "v_max_u32 %[t], %[t], %[y3]\n\t"
                    "v_add_f32_dpp %[sm], %[p1], %[p3] row_ror:8 row_mask:0xf bank_mask:0xf\n\t"
                    "v_cmp_eq_u32 %[bad], -1, %[t]\n\t"
                    "v_max3_f32 %[m], %[m], %[x3], %[sm]\n\t"
                    "s_nop 1\n\t"
                    "v_max_f32_dpp %[m], %[m], %[m] row_ror:8 row_mask:0xf bank_mask:0xf\n\t"
                    "v_mov_b32 %[t], %[m]\n\ts_nop 1\n\tv_permlane16_swap_b32 %[m], %[t]\n\tv_max_f32 %[m], %[m], %[t]\n\t"
                    "v_mov_b32 %[t], %[m]\n\ts_nop 1\n\tv_permlane32_swap_b32 %[m], %[t]\n\tv_max_f32 %[m], %[m], %[t]\n\t"
                    "v_mul_f32 %[m], %[nl2e], %[m]\n\t"
                    "v_fma_f32 %[sm], %[sm], %[l2e], %[m]\n\tv_fma_f32 %[x0], %[x0], %[l2e], %[m]\n\tv_fma_f32 %[x1], %[x1], %[l2e], %[m]\n\t"
                    "v_fma_f32 %[x2], %[x2], %[l2e], %[m]\n\tv_fma_f32 %[x3], %[x3], %[l2e], %[m]\n\t"
                    "v_exp_f32 %[sm], %[sm]\n\tv_exp_f32 %[x0], %[x0]\n\tv_exp_f32 %[x1], %[x1]\n\tv_exp_f32 %[x2], %[x2]\n\tv_exp_f32 %[x3], %[x3]\n\ts_nop 0"
                    : [x0] "=&v"(x0), [x1] "=&v"(x1), [x2] "=&v"(x2), [x3] "=&v"(x3), [sm] "=&v"(sm), [p0] "=&v"(p0), [p1] "=&v"(p1), [p2] "=&v"(p2), [p3] "=&v"(p3),
                      [m] "=&v"(m), [t] "=&v"(t), [bad] "=&s"(bad)
                    : [y0] "v"(y.x), [y1] "v"(y.y), [y2] "v"(y.z), [y3] "v"(y.w), [hi] "s"(HI), [ap] "s"(m_ap), [l2e] "s"(L2E), [nl2e] "s"(NL2E));
                const unsigned tm = pk_bf(sm, sm);
                const u32x2 pb = {pk_bf(x0, x1), pk_bf(x2, x3)};
                const u32x2 pm = {tm & m_a, tm & m_b};
                f32x4 om, oa, ob;
                asm volatile("s_nop 1\n\tv_mfma_f32_16x16x16_bf16 %0, %3, %4, 0\n\tv_mfma_f32_16x16x16_bf16 %1, %5, %7, %0\n\tv_mfma_f32_16x16x16_bf16 %2, %6, %7, %0"
                             : "=&v"(om), "=&v"(oa), "=&v"(ob) : "v"(ub), "v"(pm), "v"(v1), "v"(v2), "v"(pb));
                float l, l2;
                asm volatile(
                    "v_add_f32 %[l], %[x0], %[x1]\n\tv_add_f32 %[l2], %[x2], %[x3]\n\tv_add_f32 %[l], %[l], %[sm]\n\tv_add_f32 %[l], %[l], %[l2]\n\ts_nop 1\n\t"
                    "v_add_f32_dpp %[l], %[l], %[l] row_ror:8 row_mask:0xf bank_mask:0xf\n\t"
                    "v_mov_b32 %[l2], %[l]\n\ts_nop 1\n\tv_permlane16_swap_b32 %[l], %[l2]\n\tv_add_f32 %[l], %[l], %[l2]\n\t"
                    "v_mov_b32 %[l2], %[l]\n\ts_nop 1\n\tv_permlane32_swap_b32 %[l], %[l2]\n\tv_add_f32 %[l], %[l], %[l2]\n\t"
                    "v_rcp_f32 %[l], %[l]\n\t"
                    "v_add_f32_dpp %[r0], %[b0], %[a0] row_ror:8 row_mask:0xf bank_mask:0xf\n\tv_add_f32_dpp %[r1], %[b1], %[a1] row_ror:8 row_mask:0xf bank_mask:0xf\n\t"
                    "v_add_f32_dpp %[r2], %[b2], %[a2] row_ror:8 row_mask:0xf bank_mask:0xf\n\tv_add_f32_dpp %[r3], %[b3], %[a3] row_ror:8 row_mask:0xf bank_mask:0xf\n\t"
                    "v_mul_f32 %[r0], %[r0], %[l]\n\tv_mul_f32 %[r1], %[r1], %[l]\n\tv_mul_f32 %[r2], %[r2], %[l]\n\tv_mul_f32 %[r3], %[r3], %[l]"
                    : [l] "=&v"(l), [l2] "=&v"(l2), [r0] "=&v"(o0), [r1] "=&v"(o1_), [r2] "=&v"(o2_), [r3] "=&v"(o3)
                    : [x0] "v"(x0), [x1] "v"(x1), [x2] "v"(x2), [x3] "v"(x3), [sm] "v"(sm),
                      [a0] "v"(oa[0]), [a1] "v"(oa[1]), [a2] "v"(oa[2]), [a3] "v"(oa[3]), [b0] "v"(ob[0]), [b1] "v"(ob[1]), [b2] "v"(ob[2]), [b3] "v"(ob[3]));
                return NOCOMM == 0 && bad != 0;
            };
            if (__builtin_expect(pass(), 0)) {
                bool bad = true;
                while (bad && ++sp < cap) { asm volatile("" ::: "memory"); y = LD16(rhx, vo_yl, so_s, SC1); bad = pass(); }
                if (bad) { st_agent(ctrl + CT_ABORT, 1u); cap = 1u; }
            }
            const f32x4 o = {o0, o1_, o2_, o3};
            RSTAMP(1, s * 16 + 6);
            if (x < 8) *(f32x4*)(smem + L_R2 + ((w * 8 + x) * 4 + q) * 16) = o;
            BAR();
            RSTAMP(1, s * 16 + 7);
            if (W7) {
                const int hf = lane >> 5, i = (lane >> 2) & 7, d = lane & 3;
                f32x2 t[8];
#pragma unroll
                for (int k = 0; k < 8; ++k) t[k] = *(const f32x2*)(smem + L_R2 + ((k * 8 + i) * 4 + 2 * hf + (d >> 1)) * 16 + (d & 1) * 8);
                const f32x2 sum = ((t[0] + t[1]) + (t[2] + t[3])) + ((t[4] + t[5]) + (t[6] + t[7]));
                ST4(min(pk_bf(sum[0], sum[1]), 0xfffffffeu), rhx, vo_xs, so_n, SC1);
                if (c == 0) publish_t(so_n);
                RSTAMP(7, s * 16 + 8); RVAL(7, s * 16 + 15, (unsigned)__builtin_amdgcn_s_memrealtime());
            }
            SB();
            t_x = later(t_x, now_ticks() + D_X);
            if (q < 2) ST8(ub, rvm, vo_vm, (s * 16 + g) * 7168, SC1);
            RVAL(1, s * 16 + 11, sp + 1);
        }
        if (!BROLE) {
            wait_until(later(t_prev + K_Y, t_b2 + D_Y));
            (void)poll16(rhx, vo_yl, so_s, ctrl, cap);
            t_x = later(t_x, now_ticks() + 30 + D_X);
        }
        if (W7) {
            if (s + 2 < NSEG && s + 2 >= kv_ready) {
                for (unsigned sp = 0; sp < SPIN_CAP; ++sp) {
                    const int sg = s + 2 + (lane >> 3);
                    const unsigned long long rdy = __ballot(sg >= NSEG || ld_agent(ctrl + CT_KV(sg & 63, lane & 7)) >= KV_PER_SHARD);
                    kv_ready = s + 2 + ((rdy == ~0ull ? 64 : __builtin_ctzll(~rdy)) >> 3);
                    if (s + 2 < kv_ready) break;
                    __builtin_amdgcn_s_sleep(4);
                    if (sp + 1 == SPIN_CAP) st_agent(ctrl + CT_ABORT, 1u);
                }
            }
            wait_until(t_x);
            gather_t(s + 1, so_n, 1);
        } else {
            wait_until(t_x);
            RSTAMP(1, s * 16 + 9);
            gather_x(so_n);
            RVAL(1, s * 16 + 12, x_tries + 1);
            RSTAMP(1, s * 16 + 10);
        }
        so_s = so_n;
    }
    {
        const int s = NSEG - 1;
        BAR();
        if (!W7 && tid_opaque() == 0) add_agent(ctrl + CT_M(s - 1, c & 7), 1u);
        if (!W7) {
            const u32x2 tb = a_mfma();
            if (x < 8) ST8(tb, rkm, vo_km, (s * 16 + h) * 7168, SC1);
        } else {
#pragma unroll
            for (int ks = 0; ks < 14; ++ks) fr[ks] = *(const u32x4*)(smem + L_F + ks * 1024 + lane * 16);
        }
        if (BROLE) { const u32x2 ub = u_part(); if (q < 2) ST8(ub, rvm, vo_vm, (s * 16 + g) * 7168, SC1); }
        asm volatile("s_waitcnt vmcnt(0)" ::: "memory");
        BAR();
        if (!W7 && tid_opaque() == 0) add_agent(ctrl + CT_M(s, c & 7), 1u);
    }
}

DI void rec_role(const Params& p, char* smem, int c) {
    const int w = __builtin_amdgcn_readfirstlane(threadIdx.x >> 6);
    if (c < 56) { if (w < 7) rec_wave<true, false>(p, smem, c, w); else rec_wave<true, true>(p, smem, c, w); }
    else        { if (w < 7) rec_wave<false, false>(p, smem, c, w); else rec_wave<false, true>(p, smem, c, w); }
}

constexpr int LA_X = 0;
constexpr int LA_SM = 28672;
constexpr int LA_PX = LA_SM + 16384;
constexpr int LA_PM = LA_PX + 8192;
constexpr int LA_RED = LA_PM + 2048;
constexpr int LA_BC = LA_RED + 1024;

DI void attn_item(const Params& p, char* smem, int s, int hg) {
    const int tid = threadIdx.x, lane = tid & 63, w = __builtin_amdgcn_readfirstlane(tid >> 6);
    const int tok = lane & 31, hh = lane >> 5;
    unsigned* ctrl = (unsigned*)(p.ws + OFF_CTRL);
    const rsrc_t rkt = mk_rsrc(p.ws + OFF_KT, (unsigned)SZ_KT), rvt = mk_rsrc(p.ws + OFF_VT, (unsigned)SZ_KT);
    const rsrc_t rkm = mk_rsrc(p.ws + OFF_KM, (unsigned)SZ_KM), rvm = mk_rsrc(p.ws + OFF_VM, (unsigned)SZ_KM);
    const rsrc_t rop = mk_rsrc(p.ws + OFF_OP, (unsigned)SZ_OP);
    const rsrc_t rhd = mk_rsrc(p.ws + OFF_HD, (unsigned)(64 * SZ_SEGF)), rhn = mk_rsrc(p.ws + OFF_HNF, (unsigned)(64 * SZ_SEGF));
    {
        const char* src = p.ws + OFF_XNF + (size_t)s * SZ_SEGF;
#pragma unroll
        for (int i = 0; i < 4; ++i) { const int k = w + 8 * i; if (k < 28) GLDS(src + k * 1024 + lane * 16, smem + LA_X + k * 1024, 0); }
    }
    if (w == 0) wait_shards(ctrl, CT_KV(s, 0), KV_PER_SHARD, 2);
    asm volatile("s_waitcnt vmcnt(0)" ::: "memory");
    BAR();
    const int nt = w < 6 ? 2 : 1;
    f32x16 sx;
#pragma unroll
    for (int i = 0; i < 16; ++i) sx[i] = 0.f;
    if (w < 4) {
        u32x4 kf[28];
#pragma unroll
        for (int ks = 0; ks < 28; ++ks) kf[ks] = LD16(rkt, lane * 16, (s * 16 + 4 * hg + w) * 28672 + ks * 1024, SC1);
#pragma unroll
        for (int ks = 0; ks < 28; ++ks) sx = mfma32(kf[ks], *(const u32x4*)(smem + LA_X + ks * 1024 + lane * 16), sx);
    }
    u32x4 vf[2][8];
#pragma unroll
    for (int t = 0; t < 2; ++t)
        if (t < nt) {
#pragma unroll
            for (int k = 0; k < 8; ++k) vf[t][k] = LD16(rvt, lane * 16, (((s * 16 + 4 * hg + (k >> 1)) * 14 + w + 8 * t) * 2 + (k & 1)) * 1024, SC1);
        }
    if (w == 4) wait_shards(ctrl, CT_M(s, 0), M_PER_SHARD, s < 60 ? 4 : 1);
    BAR();
    if (w == 0 && s == 63) STAMP(400 + hg, 10);
    u32x2 vm[2][4];
#pragma unroll
    for (int t = 0; t < 2; ++t)
        if (t < nt) {
#pragma unroll
            for (int k = 0; k < 4; ++k) vm[t][k] = LD8(rvm, (32 * (w + 8 * t) + tok) * 16 + hh * 8, (s * 16 + 4 * hg + k) * 7168, SC1);
        }
    if (w >= 4) {
        const int kq = w - 4;
        u32x4 mf[7];
#pragma unroll
        for (int i = 0; i < 7; ++i) mf[i] = LD16(rkm, (hh * 8 + (tok & 7)) * 16 + (tok >> 3) * 7168, (s * 16 + 4 * hg) * 7168 + (7 * kq + i) * 256, SC1);
        f32x16 sm;
#pragma unroll
        for (int i = 0; i < 16; ++i) sm[i] = 0.f;
#pragma unroll
        for (int i = 0; i < 7; ++i) sm = mfma32(mf[i], *(const u32x4*)(smem + LA_X + (7 * kq + i) * 1024 + lane * 16), sm);
#pragma unroll
        for (int gq = 0; gq < 4; ++gq) *(f32x4*)(smem + LA_SM + ((kq * 4 + gq) * 64 + lane) * 16) = (f32x4){sm[4 * gq], sm[4 * gq + 1], sm[4 * gq + 2], sm[4 * gq + 3]};
    }
    BAR();
    if (w < 4) {
        f32x4 m4 = *(const f32x4*)(smem + LA_SM + ((0 * 4 + w) * 64 + lane) * 16);
#pragma unroll
        for (int kq = 1; kq < 4; ++kq) m4 += *(const f32x4*)(smem + LA_SM + ((kq * 4 + w) * 64 + lane) * 16);
        float mx = fmaxf(fmaxf(m4[0], m4[1]), fmaxf(m4[2], m4[3]));
#pragma unroll
        for (int i = 0; i < 16; ++i) {
            const int key = (i & 3) + 8 * (i >> 2) + 4 * hh;
            sx[i] = key <= tok ? sx[i] : -1e30f;
            mx = fmaxf(mx, sx[i]);
        }
        mx = xmax32(mx);
        const float mc = mx * LOG2E;
        float l = 0.f;
#pragma unroll
        for (int i = 0; i < 16; ++i) { sx[i] = __builtin_amdgcn_exp2f(sx[i] * LOG2E - mc); l += sx[i]; }
#pragma unroll
        for (int i = 0; i < 4; ++i) { m4[i] = __builtin_amdgcn_exp2f(m4[i] * LOG2E - mc); l += m4[i]; }
        l = xsum32(l);
        const float inv = 1.0f / l;
#pragma unroll
        for (int k = 0; k < 2; ++k) {
            u32x4 v = {pk_bf(sx[8 * k] * inv, sx[8 * k + 1] * inv), pk_bf(sx[8 * k + 2] * inv, sx[8 * k + 3] * inv),
                       pk_bf(sx[8 * k + 4] * inv, sx[8 * k + 5] * inv), pk_bf(sx[8 * k + 6] * inv, sx[8 * k + 7] * inv)};
            *(u32x4*)(smem + LA_PX + ((w * 2 + k) * 64 + lane) * 16) = v;
        }
        *(u32x2*)(smem + LA_PM + (w * 64 + lane) * 8) = (u32x2){pk_bf(m4[0] * inv, m4[1] * inv), pk_bf(m4[2] * inv, m4[3] * inv)};
    }
    BAR();
#pragma unroll
    for (int t = 0; t < 2; ++t)
        if (t < nt) {
            f32x16 o;
#pragma unroll
            for (int i = 0; i < 16; ++i) o[i] = 0.f;
#pragma unroll
            for (int k = 0; k < 8; ++k) o = mfma32(vf[t][k], *(const u32x4*)(smem + LA_PX + (k * 64 + lane) * 16), o);
#pragma unroll
            for (int pp = 0; pp < 2; ++pp) {
                const u32x2 pa = *(const u32x2*)(smem + LA_PM + ((2 * pp) * 64 + lane) * 8), pb = *(const u32x2*)(smem + LA_PM + ((2 * pp + 1) * 64 + lane) * 8);
                o = mfma32((u32x4){vm[t][2 * pp].x, vm[t][2 * pp].y, vm[t][2 * pp + 1].x, vm[t][2 * pp + 1].y}, (u32x4){pa.x, pa.y, pb.x, pb.y}, o);
            }
#pragma unroll
            for (int gq = 0; gq < 4; ++gq) {
                f32x4 v = {o[4 * gq], o[4 * gq + 1], o[4 * gq + 2], o[4 * gq + 3]};
                ST16(__builtin_bit_cast(u32x4, v), rop, lane * 16 + gq * 1024, ((s * 4 + hg) * 14 + w + 8 * t) * 4096, SC1);
            }
        }
    if (w == 0 && s == 63) STAMP(400 + hg, 11);
    asm volatile("s_waitcnt vmcnt(0)" ::: "memory");
    BAR();
    if (w == 0 && s == 63) STAMP(400 + hg, 12);
    if (w == 0) {
        if (lane == 0) add_agent(ctrl + CT_A(s), 1u);
        wait_count(ctrl, CT_A(s), 4u, 0);
    }
    BAR();
    {
        const int t8 = tid & 7, pr = tid >> 3, tk = 8 * hg + t8;
        const int cti = pr >> 2, k = (pr >> 1) & 1, h2 = pr & 1;
        const bool act = tid < 448;
        float hv[8];
        float ssum = 0.f;
        if (act) {
            const int vo = ((cti * 4 + 2 * k) * 64 + h2 * 32 + tk) * 16;
            f32x4 pa[4], pb[4];
#pragma unroll
            for (int g4 = 0; g4 < 4; ++g4) {
                pa[g4] = __builtin_bit_cast(f32x4, LD16(rop, vo, (s * 4 + g4) * 57344, SC1));
                pb[g4] = __builtin_bit_cast(f32x4, LD16(rop, vo + 1024, (s * 4 + g4) * 57344, SC1));
            }
            const uint16_t* xp = p.x + (size_t)(32 * s + tk) * DM + 32 * cti + 16 * k + 4 * h2;
            const u32x2 xa = *(const u32x2*)xp, xb = *(const u32x2*)(xp + 8);
            const f32x4 a = ((pa[0] + pa[1]) + pa[2]) + pa[3], bq = ((pb[0] + pb[1]) + pb[2]) + pb[3];
            const float xv[8] = {bf_lo(xa.x), bf_hi(xa.x), bf_lo(xa.y), bf_hi(xa.y), bf_lo(xb.x), bf_hi(xb.x), bf_lo(xb.y), bf_hi(xb.y)};
#pragma unroll
            for (int e = 0; e < 4; ++e) { hv[e] = rbf(xv[e] + rbf(a[e])); hv[4 + e] = rbf(xv[4 + e] + rbf(bq[e])); }
#pragma unroll
            for (int e = 0; e < 8; ++e) ssum += hv[e];
        } else {
#pragma unroll
            for (int e = 0; e < 8; ++e) hv[e] = 0.f;
        }
        float* red = (float*)(smem + LA_RED);
        ssum += dpp_ror8(ssum); ssum = xsum32(xsum16(ssum));
        if (lane < 8) red[w * 8 + lane] = ssum;
        BAR();
        float mean = 0.f;
#pragma unroll
        for (int q8 = 0; q8 < 7; ++q8) mean += red[q8 * 8 + t8];
        mean *= (1.0f / DM);
        float s2 = 0.f;
        if (act) {
#pragma unroll
            for (int e = 0; e < 8; ++e) { const float d = hv[e] - mean; s2 += d * d; }
        }
        s2 += dpp_ror8(s2); s2 = xsum32(xsum16(s2));
        if (lane < 8) red[64 + w * 8 + lane] = s2;
        BAR();
        float var = 0.f;
#pragma unroll
        for (int q8 = 0; q8 < 7; ++q8) var += red[64 + q8 * 8 + t8];
        var *= (1.0f / DM);
        const float mb = rbf(mean), den = rbf(sqrtf(rbf(rbf(var) + 1e-5f)));
        if (act) {
            float y[8];
#pragma unroll
            for (int e = 0; e < 8; ++e) y[e] = rbf(hv[e] - mb) / den;
            const int vo = ((2 * cti + k) * 64 + h2 * 32 + tk) * 16;
            ST16(((u32x4){pk_bf(hv[0], hv[1]), pk_bf(hv[2], hv[3]), pk_bf(hv[4], hv[5]), pk_bf(hv[6], hv[7])}), rhd, vo, s * (int)SZ_SEGF, SC1);
            ST16(((u32x4){pk_bf(y[0], y[1]), pk_bf(y[2], y[3]), pk_bf(y[4], y[5]), pk_bf(y[6], y[7])}), rhn, vo, s * (int)SZ_SEGF, SC1);
        }
        asm volatile("s_waitcnt vmcnt(0)" ::: "memory");
        BAR();
        if (w == 0 && s == 63) STAMP(404, 10 + hg);
        if (tid_opaque() == 0) add_agent(ctrl + CT_H(s), 1u);
    }
    BAR();
}

constexpr int K2_BC = 122880;
constexpr int K2_LDS = K2_BC + 256;

__global__ void __launch_bounds__(512, 2) k2_main(Params p) {
    extern __shared__ __attribute__((aligned(16))) char smem[];
    const int tid = threadIdx.x;
    unsigned* ctrl = (unsigned*)(p.ws + OFF_CTRL);
    volatile LAS unsigned* bc = (volatile LAS unsigned*)(smem + K2_BC);
    if (tid == 0) {
        const unsigned xc = xcc_id();
        const unsigned r = add_agent(ctrl + CT_TICK(xc), 1u);
        unsigned role;
        if ((xc == 0u || xc == 2u) && r < 32u) role = (xc >> 1) * 32u + r;
        else role = N_REC + add_agent(ctrl + CT_WTICK, 1u);
        asm volatile("s_waitcnt vmcnt(0)" ::: "memory");
        add_agent(ctrl + CT_TOTAL, 1u);
        if (role >= N_WG) {
            unsigned sp = 0;
            while (ld_agent(ctrl + CT_TOTAL) < gridDim.x && ++sp < SPIN_CAP) __builtin_amdgcn_s_sleep(8);
            const unsigned c0 = min(ld_agent(ctrl + CT_TICK(0)), 32u), c2 = min(ld_agent(ctrl + CT_TICK(2)), 32u);
            const unsigned mi = role - N_WG;
            role = mi < 32u - c0 ? c0 + mi : 32u + c2 + (mi - (32u - c0));
        }
        bc[0] = role;
    }
    __syncthreads();
    const int role = __builtin_amdgcn_readfirstlane(bc[0]);
    __syncthreads();
    if (role < N_REC) { rec_role(p, smem, role); return; }
    if (role < N_REC + N_GEMM) { gemm448<0>(p, smem, role - N_REC, 0, 32); __syncthreads(); }
    else if (role < N_REC + N_GEMM + N_F1) { const int i = role - N_REC - N_GEMM; ffn1_role(p, smem, i >> 1, i & 1); return; }
    else if (role < N_REC + N_GEMM + N_F1 + N_F2) { const int i = role - N_REC - N_GEMM - N_F1; ffn2_role(p, smem, i >> 1, i & 1); return; }
    for (;;) {
        if (tid_opaque() == 0) bc[0] = add_agent(ctrl + CT_ATICK, 1u);
        __syncthreads();
        const int item = __builtin_amdgcn_readfirstlane(bc[0]);
        __syncthreads();
        if (item >= 4 * NSEG) break;
        if (tid_opaque() < 64) STAMP(256 + item, 0);
        attn_item(p, smem, item >> 2, item & 3);
        if (tid_opaque() < 64) STAMP(256 + item, 1);
    }
}

extern "C" void kernel_launch(void* const* d_in, const int* in_sizes, int n_in, void* d_out, int out_size, void* d_ws, size_t ws_size, hipStream_t stream) {
    Params p;
    p.x = (const uint16_t*)d_in[0]; p.mem0 = (const uint16_t*)d_in[1];
    p.wq = (const uint16_t*)d_in[2]; p.wk = (const uint16_t*)d_in[3]; p.wv = (const uint16_t*)d_in[4]; p.wo = (const uint16_t*)d_in[5];
    p.w1 = (const uint16_t*)d_in[8]; p.b1 = (const uint16_t*)d_in[9]; p.w2 = (const uint16_t*)d_in[10]; p.b2 = (const uint16_t*)d_in[11];
    p.out = (uint16_t*)d_out; p.ws = (char*)d_ws;
    uintptr_t hx = ((uintptr_t)d_ws + OFF_END + ((size_t)2 << 20) - 1) & ~(uintptr_t)(((size_t)2 << 20) - 1);
    p.hx = (char*)hx;
    (void)hipFuncSetAttribute((const void*)k2_main, hipFuncAttributeMaxDynamicSharedMemorySize, K2_LDS);
    (void)hipFuncSetAttribute((const void*)k1_prep, hipFuncAttributeMaxDynamicSharedMemorySize, K1_LDS);
    hipLaunchKernelGGL(k1_prep, dim3(K1_GRID), dim3(256), K1_LDS, stream, p);
    hipLaunchKernelGGL(k2_main, dim3(N_WG), dim3(512), K2_LDS, stream, p);
}
```

```cpp
#include <hip/hip_runtime.h>
#include <hip/hip_bf16.h>
#include <stdint.h>

typedef __bf16 bf16x8 __attribute__((ext_vector_type(8)));
typedef __bf16 bf16x2 __attribute__((ext_vector_type(2)));
typedef short s16x4 __attribute__((ext_vector_type(4)));
typedef float f32x2 __attribute__((ext_vector_type(2)));
typedef float f32x4 __attribute__((ext_vector_type(4)));
typedef float f32x16 __attribute__((ext_vector_type(16)));
typedef unsigned u32x2 __attribute__((ext_vector_type(2)));
typedef unsigned u32x4 __attribute__((ext_vector_type(4)));
#define LAS __attribute__((address_space(3)))
#define GAS __attribute__((address_space(1)))
#define DI __device__ __forceinline__
typedef __amdgpu_buffer_rsrc_t rsrc_t;

constexpr int DM = 448, NH = 16, DH = 256, SEQ = 2048, NSEG = 64, FF = 1792;

constexpr size_t OFF_CTRL = 0;
constexpr size_t SZ_CTRL = 262144;
constexpr size_t OFF_XNF = OFF_CTRL + SZ_CTRL;
constexpr size_t SZ_SEGF = 28 * 1024;
constexpr size_t OFF_WCT = OFF_XNF + 64 * SZ_SEGF;
constexpr size_t SZ_WCT = (size_t)448 * 56 * 512;
constexpr size_t OFF_KT = OFF_WCT + SZ_WCT;
constexpr size_t SZ_KT = (size_t)64 * 16 * 28672;
constexpr size_t OFF_VT = OFF_KT + SZ_KT;
constexpr size_t OFF_KM = OFF_VT + SZ_KT;
constexpr size_t SZ_KM = (size_t)64 * 16 * 7168;
constexpr size_t OFF_VM = OFF_KM + SZ_KM;
constexpr size_t OFF_OP = OFF_VM + SZ_KM;
constexpr size_t SZ_OP = (size_t)64 * 4 * 57344;
constexpr size_t OFF_HD = OFF_OP + SZ_OP;
constexpr size_t OFF_HNF = OFF_HD + 64 * SZ_SEGF;
constexpr size_t OFF_RF = OFF_HNF + 64 * SZ_SEGF;
constexpr size_t SZ_RF = (size_t)64 * 112 * 1024;
constexpr size_t OFF_END = OFF_RF + SZ_RF;
constexpr size_t SZ_HX = (size_t)4 << 20;

#define CT_TICK(x) (64 * (x))
#define CT_WTICK (64 * 16)
#define CT_TOTAL (64 * 17)
#define CT_ATICK (64 * 18)
#define CT_ABORT (64 * 19)
#define CT_PROBE(c) (57344 + (c) * 32)
#define CT_START(sh) (64 * (22 + (sh)))
#define CT_A(s) (64 * (32 + (s)))
#define CT_H(s) (49152 + (s) * 32)
#define CT_R(s) (49152 + 2048 + (s) * 32)
#define CT_KV(s, sh) (8192 + ((s) * 8 + (sh)) * 32)
#define CT_M(s, sh) (8192 + 16384 + ((s) * 8 + (sh)) * 32)
constexpr unsigned KV_PER_SHARD = 14, M_PER_SHARD = 8;

#define STAMP(wg, idx) do {} while (0)
#define RSTAMP(wv, idx) do {} while (0)
#define RVAL(wv, idx, val) do {} while (0)
constexpr int N_REC = 64, N_GEMM = 112, N_F1 = 28, N_F2 = 28, N_WG = 256;
constexpr unsigned SPIN_CAP = 1u << 15;
constexpr float LOG2E = 1.4426950408889634f;

struct Params {
    const uint16_t* x; const uint16_t* mem0; const uint16_t* wq; const uint16_t* wk; const uint16_t* wv; const uint16_t* wo;
    const uint16_t* w1; const uint16_t* b1; const uint16_t* w2; const uint16_t* b2;
    uint16_t* out; char* ws; char* hx;
};

DI float bf_lo(unsigned v) { return __builtin_bit_cast(float, v << 16); }
DI float bf_hi(unsigned v) { return __builtin_bit_cast(float, v & 0xffff0000u); }
DI unsigned pk_bf(float a, float b) { bf16x2 r = {(__bf16)a, (__bf16)b}; return __builtin_bit_cast(unsigned, r); }
DI float rbf(float a) { return bf_lo(pk_bf(a, a)); }
DI float dpp_ror8(float v) { return __builtin_bit_cast(float, __builtin_amdgcn_update_dpp(0, __builtin_bit_cast(int, v), 0x128, 0xf, 0xf, true)); }
DI unsigned fb(float v) { return __builtin_bit_cast(unsigned, v); }
DI float bfl(unsigned v) { return __builtin_bit_cast(float, v); }
DI float xsum32(float v) { auto r = __builtin_amdgcn_permlane32_swap(fb(v), fb(v), false, false); return bfl(r[0]) + bfl(r[1]); }
DI float xsum16(float v) { auto r = __builtin_amdgcn_permlane16_swap(fb(v), fb(v), false, false); return bfl(r[0]) + bfl(r[1]); }
DI float xmax32(float v) { auto r = __builtin_amdgcn_permlane32_swap(fb(v), fb(v), false, false); return fmaxf(bfl(r[0]), bfl(r[1])); }
DI float xmax16(float v) { auto r = __builtin_amdgcn_permlane16_swap(fb(v), fb(v), false, false); return fmaxf(bfl(r[0]), bfl(r[1])); }
DI f32x4 mfma16(u32x4 a, u32x4 b, f32x4 c) { return __builtin_amdgcn_mfma_f32_16x16x32_bf16(__builtin_bit_cast(bf16x8, a), __builtin_bit_cast(bf16x8, b), c, 0, 0, 0); }
DI f32x4 mfma16k16(u32x2 a, u32x2 b, f32x4 c) { return __builtin_amdgcn_mfma_f32_16x16x16bf16_1k(__builtin_bit_cast(s16x4, a), __builtin_bit_cast(s16x4, b), c, 0, 0, 0); }
DI f32x16 mfma32(u32x4 a, u32x4 b, f32x16 c) { return __builtin_amdgcn_mfma_f32_32x32x16_bf16(__builtin_bit_cast(bf16x8, a), __builtin_bit_cast(bf16x8, b), c, 0, 0, 0); }
DI rsrc_t mk_rsrc(const void* p, unsigned bytes) { return __builtin_amdgcn_make_buffer_rsrc((void*)p, 0, bytes, 0x00020000); }
#define LD16(r, vo, so, aux) __builtin_amdgcn_raw_buffer_load_b128((r), (vo), (so), (aux))
#define LD8(r, vo, so, aux) __builtin_amdgcn_raw_buffer_load_b64((r), (vo), (so), (aux))
#define ST16(v, r, vo, so, aux) do { const u32x4 st16_v_ = (v); __builtin_amdgcn_raw_buffer_store_b128(st16_v_, (r), (vo), (so), (aux)); asm volatile("s_nop 1" :: "v"(st16_v_)); } while (0)
#define ST8(v, r, vo, so, aux) __builtin_amdgcn_raw_buffer_store_b64((v), (r), (vo), (so), (aux))
#define ST4(v, r, vo, so, aux) __builtin_amdgcn_raw_buffer_store_b32((v), (r), (vo), (so), (aux))
constexpr int SC1 = 16;
#define BAR() do { asm volatile("s_waitcnt lgkmcnt(0)" ::: "memory"); __builtin_amdgcn_s_barrier(); asm volatile("" ::: "memory"); } while (0)
#define GLDS(src, dst, aux) __builtin_amdgcn_global_load_lds((const GAS void*)(src), (LAS void*)(dst), 16, 0, (aux))
DI unsigned ld_agent(const unsigned* p) { return __hip_atomic_load((const GAS unsigned*)p, __ATOMIC_RELAXED, __HIP_MEMORY_SCOPE_AGENT); }
DI void st_agent(unsigned* p, unsigned v) { __hip_atomic_store((GAS unsigned*)p, v, __ATOMIC_RELAXED, __HIP_MEMORY_SCOPE_AGENT); }
DI unsigned add_agent(unsigned* p, unsigned v) { return __hip_atomic_fetch_add((GAS unsigned*)p, v, __ATOMIC_RELAXED, __HIP_MEMORY_SCOPE_AGENT); }
DI int tid_opaque() { int t = threadIdx.x; asm volatile("" : "+v"(t)); return t; }
DI unsigned xcc_id() { return (unsigned)__builtin_amdgcn_s_getreg(20 | (3 << 11)) & 0xfu; }
DI float fdiv(float a, float rb) { return a * rb; }
DI float frcp(float b) { const float r = __builtin_amdgcn_rcpf(b); return __builtin_fmaf(__builtin_fmaf(-b, r, 1.0f), r, r); }
DI float wave_sum(float v) {
    v += dpp_ror8(v);
    v += __builtin_bit_cast(float, __builtin_amdgcn_update_dpp(0, __builtin_bit_cast(int, v), 0x124, 0xf, 0xf, true));
    v += __builtin_bit_cast(float, __builtin_amdgcn_update_dpp(0, __builtin_bit_cast(int, v), 0x122, 0xf, 0xf, true));
    v += __builtin_bit_cast(float, __builtin_amdgcn_update_dpp(0, __builtin_bit_cast(int, v), 0x121, 0xf, 0xf, true));
    return xsum32(xsum16(v));
}
DI void wait_count(unsigned* ctrl, int word, unsigned target, int slp) {
    for (unsigned sp = 0; sp < SPIN_CAP; ++sp) {
        unsigned v = __builtin_amdgcn_readfirstlane(ld_agent(ctrl + word));
        if (v >= target) return;
        __builtin_amdgcn_s_sleep(2);
        for (int i = 0; i < slp; ++i) __builtin_amdgcn_s_sleep(8);
        if ((sp & 255u) == 255u && __builtin_amdgcn_readfirstlane(ld_agent(ctrl + CT_ABORT))) return;
    }
    st_agent(ctrl + CT_ABORT, 1u);
}

DI void wait_shards(unsigned* ctrl, int word, unsigned target, int slp) {
    const int lane = threadIdx.x & 7;
    for (unsigned sp = 0; sp < SPIN_CAP; ++sp) {
        if (!__any(ld_agent(ctrl + word + lane * 32) < target)) return;
        __builtin_amdgcn_s_sleep(2);
        for (int i = 0; i < slp; ++i) __builtin_amdgcn_s_sleep(8);
        if ((sp & 63u) == 63u && __builtin_amdgcn_readfirstlane(ld_agent(ctrl + CT_ABORT))) return;
    }
    st_agent(ctrl + CT_ABORT, 1u);
}

constexpr int K1_GRID = 512;
constexpr int K1_Q = 2 * 112 * 128;
constexpr int K1_LDS = 2 * K1_Q + 1024;

__global__ void __launch_bounds__(256, 2) k1_prep(Params p) {
    extern __shared__ __attribute__((aligned(16))) char smem[];
    const int b = blockIdx.x, tid = threadIdx.x, lane = tid & 63, w = __builtin_amdgcn_readfirstlane(tid >> 6);
    {
        const int m = (b & 7) + 8 * (b >> 7), tr = (b >> 5) & 3, tc = (b >> 3) & 3, hd = m & 15;
        const bool isA = m < 16;
        const uint16_t* arow = (isA ? p.wk : p.wv) + (size_t)(112 * tr) * 4096 + hd * 256;
        const uint16_t* brow = p.wq + (size_t)(112 * tc) * 4096 + hd * 256;
        const uint16_t* bcol = p.wo + (size_t)hd * 256 * 448 + 112 * tc;
        auto stage = [&](int kq, int slot) {
            char* base = smem + slot * K1_Q;
#pragma unroll
            for (int i = 0; i < 4; ++i) {
                const int j = w + 4 * i;
                if (j < 14) {
                    const int r = 8 * j + (lane >> 3), c = (lane & 7) ^ (r & 7);
                    GLDS(arow + (size_t)r * 4096 + kq * 64 + c * 8, base + j * 1024, 0);
                    if (isA) GLDS(brow + (size_t)r * 4096 + kq * 64 + c * 8, base + 14336 + j * 1024, 0);
                    else { const int ch = 64 * j + lane, e = ch / 14; GLDS(bcol + (size_t)(kq * 64 + e) * 448 + (ch - 14 * e) * 8, base + 14336 + j * 1024, 0); }
                }
            }
        };
        stage(0, 0); stage(1, 1);
        const int row = 4 * b + w;
        u32x4 raw = {0u, 0u, 0u, 0u};
        if (lane < 56) raw = *(const u32x4*)(p.x + (size_t)row * DM + lane * 8);
        if (tid < 32) *(u32x4*)(p.ws + OFF_CTRL + ((size_t)b * 32 + tid) * 16) = (u32x4){0u, 0u, 0u, 0u};
        {
            float v[8];
            v[0] = bf_lo(raw.x); v[1] = bf_hi(raw.x); v[2] = bf_lo(raw.y); v[3] = bf_hi(raw.y);
            v[4] = bf_lo(raw.z); v[5] = bf_hi(raw.z); v[6] = bf_lo(raw.w); v[7] = bf_hi(raw.w);
            float s = 0.f;
#pragma unroll
            for (int e = 0; e < 8; ++e) s += v[e];
            const float mean = wave_sum(s) * (1.0f / DM);
            float s2 = 0.f;
            if (lane < 56) {
#pragma unroll
                for (int e = 0; e < 8; ++e) { const float d = v[e] - mean; s2 += d * d; }
            }
            const float var = wave_sum(s2) * (1.0f / DM);
            const float mb = rbf(mean);
            const float den = rbf(sqrtf(rbf(rbf(var) + 1e-5f)));
            if (lane < 56) {
                float y[8];
                const float rden = frcp(den);
#pragma unroll
                for (int e = 0; e < 8; ++e) y[e] = fdiv(rbf(v[e] - mb), rden);
                const u32x4 o = {pk_bf(y[0], y[1]), pk_bf(y[2], y[3]), pk_bf(y[4], y[5]), pk_bf(y[6], y[7])};
                *(u32x4*)(p.ws + OFF_XNF + (size_t)(row >> 5) * SZ_SEGF + ((lane >> 1) * 64 + (lane & 1) * 32 + (row & 31)) * 16) = o;
            }
        }
        f32x4 acc[2][7];
#pragma unroll
        for (int i = 0; i < 2; ++i)
#pragma unroll
            for (int j = 0; j < 7; ++j) acc[i][j] = (f32x4){0.f, 0.f, 0.f, 0.f};
        const int x = lane & 15, q = lane >> 4;
        const int nst = w < 2 ? 8 : 6;
#pragma unroll
        for (int kq = 0; kq < 4; ++kq) {
            if (kq < 3) { if (nst == 8) asm volatile("s_waitcnt vmcnt(8)" ::: "memory"); else asm volatile("s_waitcnt vmcnt(6)" ::: "memory"); }
            else asm volatile("s_waitcnt vmcnt(0)" ::: "memory");
            BAR();
            const char* As = smem + (kq & 1) * K1_Q; const char* Bs = As + 14336;
#pragma unroll
            for (int ks = 0; ks < 2; ++ks) {
                u32x4 bf[7], af[2];
#pragma unroll
                for (int j = 0; j < 7; ++j) {
                    if (isA) bf[j] = *(const u32x4*)(Bs + (16 * j + x) * 128 + (((4 * ks + q) ^ (x & 7)) * 16));
                    else {
                        const int r0 = 32 * ks + 8 * q + (x >> 2), cb = (16 * j + 4 * (x & 3)) * 2;
                        const s16x4 lo = __builtin_amdgcn_ds_read_tr16_b64_v4i16((LAS s16x4*)(Bs + r0 * 224 + cb));
                        const s16x4 hi = __builtin_amdgcn_ds_read_tr16_b64_v4i16((LAS s16x4*)(Bs + (r0 + 4) * 224 + cb));
                        const u32x2 l2 = __builtin_bit_cast(u32x2, lo), h2 = __builtin_bit_cast(u32x2, hi);
                        bf[j] = (u32x4){l2.x, l2.y, h2.x, h2.y};
                    }
                }
#pragma unroll
                for (int i = 0; i < 2; ++i) { const int rt = w + 4 * i < 7 ? w + 4 * i : 6; af[i] = *(const u32x4*)(As + (16 * rt + x) * 128 + (((4 * ks + q) ^ (x & 7)) * 16)); }
#pragma unroll
                for (int i = 0; i < 2; ++i)
#pragma unroll
                    for (int j = 0; j < 7; ++j) acc[i][j] = mfma16(af[i], bf[j], acc[i][j]);
            }
            if (kq + 2 < 4) { BAR(); stage(kq + 2, kq & 1); }
        }
        const float sc = isA ? 0.0625f : 1.0f;
        char* wct = p.ws + OFF_WCT;
#pragma unroll
        for (int i = 0; i < 2; ++i) {
            const int rt = w + 4 * i;
            if (rt < 7) {
#pragma unroll
                for (int j = 0; j < 7; ++j) {
                    const int np = (isA ? 0 : 7168) + hd * 448 + 112 * tc + 16 * j + x;
                    const int k = 112 * tr + 16 * rt + 4 * q;
                    const u32x2 v = {pk_bf(acc[i][j][0] * sc, acc[i][j][1] * sc), pk_bf(acc[i][j][2] * sc, acc[i][j][3] * sc)};
                    *(u32x2*)(wct + ((size_t)((np >> 5) * 56 + (k >> 3)) * 32 + (np & 31)) * 16 + ((k >> 2) & 1) * 8) = v;
                }
            }
        }
    }
}

constexpr int GEMM_LDS = 2 * 2 * 28 * 1024;

template <int MODE>
DI void gemm448(const Params& p, char* smem, int slice, int seg0, int npair) {
    const int tid = threadIdx.x, lane = tid & 63, w = __builtin_amdgcn_readfirstlane(tid >> 6);
    const int ct = w & 3, th = w >> 2, T = 4 * slice + ct;
    unsigned* ctrl = (unsigned*)(p.ws + OFF_CTRL);
    const char* src = p.ws + (MODE == 0 ? OFF_XNF : OFF_HNF) + (size_t)seg0 * SZ_SEGF;
    u32x4 wf[28];
    {
        const rsrc_t rw = mk_rsrc(p.ws + OFF_WCT, (unsigned)SZ_WCT);
#pragma unroll
        for (int ks = 0; ks < 28; ++ks) wf[ks] = LD16(rw, lane * 16 + (ks & 3) * 1024, T * 28672 + (ks & ~3) * 1024, 0);
    }
#pragma unroll
    for (int i = 0; i < 7; ++i) GLDS(src + (w * 7 + i) * 1024 + lane * 16, smem + (w * 7 + i) * 1024, 0);
    asm volatile("s_waitcnt vmcnt(0)" ::: "memory");
#pragma unroll
    for (int i = 0; i < 7; ++i) GLDS(src + (size_t)57344 + (w * 7 + i) * 1024 + lane * 16, smem + 57344 + (w * 7 + i) * 1024, 0);
    if (MODE == 0 && w == 0) STAMP(64 + slice, 1);
    const bool isK = (MODE == 0) && (T < 224);
    const int Th = isK ? T : T - 224, hd = Th / 14, ctile = Th % 14;
    const rsrc_t ro = (MODE == 0) ? mk_rsrc(p.ws + (isK ? OFF_KT : OFF_VT), (unsigned)SZ_KT) : mk_rsrc(p.ws + OFF_RF, (unsigned)SZ_RF);
    u32x2 bias[4];
    if (MODE == 1) {
#pragma unroll
        for (int g = 0; g < 4; ++g) bias[g] = *(const u32x2*)(p.b1 + 32 * T + 8 * g + 4 * (lane >> 5));
    }
    for (int pr = 0; pr < npair; ++pr) {
        if (pr >= 1) {
            if (pr + 1 < npair) asm volatile("s_waitcnt vmcnt(7)" ::: "memory"); else asm volatile("s_waitcnt vmcnt(0)" ::: "memory");
        }
        BAR();
        if (MODE == 0 && w == 0) STAMP(64 + slice, 2 + pr);
        if (MODE == 0 && pr >= 2 && tid_opaque() < 2) add_agent(ctrl + CT_KV(seg0 + 2 * (pr - 1) + tid, slice & 7), 1u);
        const char* xs = smem + (pr & 1) * 57344 + th * 28672 + lane * 16;
        f32x16 acc;
#pragma unroll
        for (int i = 0; i < 16; ++i) acc[i] = 0.f;
        if (MODE == 0 && !isK) {
#pragma unroll
            for (int ks = 0; ks < 28; ++ks) acc = mfma32(*(const u32x4*)(xs + ks * 1024), wf[ks], acc);
        } else {
#pragma unroll
            for (int ks = 0; ks < 28; ++ks) acc = mfma32(wf[ks], *(const u32x4*)(xs + ks * 1024), acc);
        }
        const int s = seg0 + 2 * pr + th;
        if (MODE == 0) {
            unsigned pk[8];
#pragma unroll
            for (int i = 0; i < 8; ++i) pk[i] = pk_bf(acc[2 * i], acc[2 * i + 1]);
            if (isK) {
#pragma unroll
                for (int k = 0; k < 2; ++k) {
                    auto r0 = __builtin_amdgcn_permlane32_swap(pk[4 * k], pk[4 * k + 2], false, false);
                    auto r1 = __builtin_amdgcn_permlane32_swap(pk[4 * k + 1], pk[4 * k + 3], false, false);
                    u32x4 v = {r0[0], r1[0], r0[1], r1[1]};
                    ST16(v, ro, lane * 16 + k * 1024, ((s * 16 + hd) * 56 + 4 * ctile) * 512, SC1);
                }
            } else {
#pragma unroll
                for (int k = 0; k < 2; ++k) {
                    u32x4 v = {pk[4 * k], pk[4 * k + 1], pk[4 * k + 2], pk[4 * k + 3]};
                    ST16(v, ro, lane * 16 + k * 1024, ((s * 16 + hd) * 14 + ctile) * 2048, SC1);
                }
            }
        } else {
            unsigned pk[8];
#pragma unroll
            for (int g = 0; g < 4; ++g) {
                float bv[4] = {bf_lo(bias[g].x), bf_hi(bias[g].x), bf_lo(bias[g].y), bf_hi(bias[g].y)};
                float r[4];
#pragma unroll
                for (int e = 0; e < 4; ++e) { float t = rbf(rbf(acc[4 * g + e]) + bv[e]); t = t > 0.f ? t : 0.f; r[e] = t * t; }
                pk[2 * g] = pk_bf(r[0], r[1]); pk[2 * g + 1] = pk_bf(r[2], r[3]);
            }
#pragma unroll
            for (int k = 0; k < 2; ++k) {
                u32x4 v = {pk[4 * k], pk[4 * k + 1], pk[4 * k + 2], pk[4 * k + 3]};
                ST16(v, ro, lane * 16 + k * 1024, (s * 112 + 2 * T) * 1024, 0);
            }
        }
        if (MODE == 0 && pr == 0) {
            asm volatile("s_waitcnt vmcnt(0)" ::: "memory");
            BAR();
            if (tid_opaque() < 2) add_agent(ctrl + CT_KV(seg0 + tid, slice & 7), 1u);
        }
        if (pr + 2 < npair) {
            BAR();
#pragma unroll
            for (int i = 0; i < 7; ++i) GLDS(src + (size_t)(pr + 2) * 57344 + (w * 7 + i) * 1024 + lane * 16, smem + (pr & 1) * 57344 + (w * 7 + i) * 1024, 0);
        }
    }
    if (MODE == 0) {
        asm volatile("s_waitcnt vmcnt(0)" ::: "memory");
        BAR();
        if (tid_opaque() < 2) add_agent(ctrl + CT_KV(seg0 + 2 * (npair - 1) + tid, slice & 7), 1u);
    }
}

DI void ffn1_role(const Params& p, char* smem, int slice, int par) {
    const int tid = threadIdx.x, lane = tid & 63, w = __builtin_amdgcn_readfirstlane(tid >> 6);
    const int ct = w & 3, kh = w >> 2, T = 4 * slice + ct, hh = lane >> 5;
    unsigned* ctrl = (unsigned*)(p.ws + OFF_CTRL);
    u32x4 wf[14];
    {
#pragma unroll
        for (int i = 0; i < 14; ++i) {
            const int ch = tid + 512 * i;
            *(u32x4*)(smem + (ch >> 4) * 272 + (ch & 15) * 16) = *(const u32x4*)(p.w1 + (size_t)(ch >> 4) * FF + 128 * slice + (ch & 15) * 8);
        }
        __syncthreads();
        const int blk = (lane >> 4) & 1, a = (lane & 15) >> 2, pp = lane & 3;
#pragma unroll
        for (int ks = 0; ks < 14; ++ks) {
            const int r0 = 16 * (14 * kh + ks) + 4 * hh + a, cb = (32 * ct + 16 * blk + 4 * pp) * 2;
            const s16x4 lo = __builtin_amdgcn_ds_read_tr16_b64_v4i16((LAS s16x4*)(smem + r0 * 272 + cb));
            const s16x4 hi = __builtin_amdgcn_ds_read_tr16_b64_v4i16((LAS s16x4*)(smem + (r0 + 8) * 272 + cb));
            const u32x2 l2 = __builtin_bit_cast(u32x2, lo), h2 = __builtin_bit_cast(u32x2, hi);
            wf[ks] = (u32x4){l2.x, l2.y, h2.x, h2.y};
        }
        __syncthreads();
    }
    u32x2 bias[4];
#pragma unroll
    for (int g = 0; g < 4; ++g) bias[g] = *(const u32x2*)(p.b1 + 32 * T + 8 * g + 4 * hh);
    const rsrc_t ro = mk_rsrc(p.ws + OFF_RF, (unsigned)SZ_RF);
    char* red = smem + 28672;
    for (int s = par; s < NSEG; s += 2) {
        if (w == 0) wait_count(ctrl, CT_H(s), 4u, s >= 60 ? 0 : 4);
        BAR();
        if (w == 0 && slice == 0 && s >= 62) STAMP(300 + s, 2);
        {
            const char* src = p.ws + OFF_HNF + (size_t)s * SZ_SEGF;
#pragma unroll
            for (int i = 0; i < 4; ++i) { const int k = w + 8 * i; if (k < 28) GLDS(src + k * 1024 + lane * 16, smem + k * 1024, SC1); }
        }
        asm volatile("s_waitcnt vmcnt(0)" ::: "memory");
        BAR();
        f32x16 acc;
#pragma unroll
        for (int i = 0; i < 16; ++i) acc[i] = 0.f;
        u32x4 xf[14];
#pragma unroll
        for (int ks = 0; ks < 14; ++ks) xf[ks] = *(const u32x4*)(smem + (14 * kh + ks) * 1024 + lane * 16);
        __builtin_amdgcn_sched_barrier(0);
#pragma unroll
        for (int ks = 0; ks < 14; ++ks) acc = mfma32(wf[ks], xf[ks], acc);
        if (kh == 1) {
#pragma unroll
            for (int g = 0; g < 4; ++g) *(f32x4*)(red + ((ct * 4 + g) * 64 + lane) * 16) = (f32x4){acc[4 * g], acc[4 * g + 1], acc[4 * g + 2], acc[4 * g + 3]};
        }
        BAR();
        if (kh == 0) {
            unsigned pk[8];
#pragma unroll
            for (int g = 0; g < 4; ++g) {
                const f32x4 o2 = *(const f32x4*)(red + ((ct * 4 + g) * 64 + lane) * 16);
                const float bv[4] = {bf_lo(bias[g].x), bf_hi(bias[g].x), bf_lo(bias[g].y), bf_hi(bias[g].y)};
                float r[4];
#pragma unroll
                for (int e = 0; e < 4; ++e) { float t = rbf(rbf(acc[4 * g + e] + o2[e]) + bv[e]); t = t > 0.f ? t : 0.f; r[e] = t * t; }
                pk[2 * g] = pk_bf(r[0], r[1]); pk[2 * g + 1] = pk_bf(r[2], r[3]);
            }
#pragma unroll
            for (int k = 0; k < 2; ++k) ST16(((u32x4){pk[4 * k], pk[4 * k + 1], pk[4 * k + 2], pk[4 * k + 3]}), ro, lane * 16 + k * 1024, (s * 112 + 2 * T) * 1024, SC1);
            asm volatile("s_waitcnt vmcnt(0)" ::: "memory");
        }
        BAR();
        if (w == 0 && slice == 0 && s >= 62) STAMP(300 + s, 3);
        if (tid_opaque() == 0) add_agent(ctrl + CT_R(s), 1u);
    }
}
DI void ffn2_role(const Params& p, char* smem, int ctile, int par) {
    const int tid = threadIdx.x, lane = tid & 63, w = __builtin_amdgcn_readfirstlane(tid >> 6);
    const int tok = lane & 31, hh = lane >> 5;
    unsigned* ctrl = (unsigned*)(p.ws + OFF_CTRL);
    u32x4 wf[14];
    {
#pragma unroll
        for (int i = 0; i < 14; ++i) {
            const int ch = tid + 512 * i;
            *(u32x4*)(smem + ch * 16) = *(const u32x4*)(p.w2 + (size_t)(ch >> 2) * DM + 32 * ctile + (ch & 3) * 8);
        }
        __syncthreads();
        const int blk = (lane >> 4) & 1, a = (lane & 15) >> 2, pp = lane & 3;
#pragma unroll
        for (int ks = 0; ks < 14; ++ks) {
            const int r0 = 16 * (14 * w + ks) + 4 * hh + a, cb = (16 * blk + 4 * pp) * 2;
            const s16x4 lo = __builtin_amdgcn_ds_read_tr16_b64_v4i16((LAS s16x4*)(smem + r0 * 64 + cb));
            const s16x4 hi = __builtin_amdgcn_ds_read_tr16_b64_v4i16((LAS s16x4*)(smem + (r0 + 8) * 64 + cb));
            const u32x2 l2 = __builtin_bit_cast(u32x2, lo), h2 = __builtin_bit_cast(u32x2, hi);
            wf[ks] = (u32x4){l2.x, l2.y, h2.x, h2.y};
        }
        __syncthreads();
    }
    const u32x2 br = *(const u32x2*)(p.b2 + 32 * ctile + 8 * (w & 3) + 4 * hh);
    const rsrc_t rr = mk_rsrc(p.ws + OFF_RF, (unsigned)SZ_RF), rh = mk_rsrc(p.ws + OFF_HD, (unsigned)(64 * SZ_SEGF));
    for (int s = par; s < NSEG; s += 2) {
        if (w == 0) wait_count(ctrl, CT_R(s), 14u, s >= 60 ? 0 : 4);
        BAR();
        if (w == 0 && ctile == 0 && s >= 62) STAMP(300 + s, 4);
        u32x4 xf[14];
#pragma unroll
        for (int ks = 0; ks < 14; ++ks) xf[ks] = LD16(rr, lane * 16, (s * 112 + 14 * w + ks) * 1024, SC1);
        u32x2 hr = {0u, 0u};
        if (w < 4) hr = LD8(rh, lane * 16 + (w & 1) * 8, s * (int)SZ_SEGF + (2 * ctile + (w >> 1)) * 1024, SC1);
        f32x16 acc;
#pragma unroll
        for (int i = 0; i < 16; ++i) acc[i] = 0.f;
#pragma unroll
        for (int ks = 0; ks < 14; ++ks) acc = mfma32(wf[ks], xf[ks], acc);
#pragma unroll
        for (int g = 0; g < 4; ++g) *(f32x4*)(smem + ((g * 8 + w) * 64 + lane) * 16) = (f32x4){acc[4 * g], acc[4 * g + 1], acc[4 * g + 2], acc[4 * g + 3]};
        BAR();
        if (w < 4) {
            f32x4 t[8];
#pragma unroll
            for (int k = 0; k < 8; ++k) t[k] = *(const f32x4*)(smem + ((w * 8 + k) * 64 + lane) * 16);
            const f32x4 o = ((t[0] + t[1]) + (t[2] + t[3])) + ((t[4] + t[5]) + (t[6] + t[7]));
            const float bv[4] = {bf_lo(br.x), bf_hi(br.x), bf_lo(br.y), bf_hi(br.y)};
            const float hv[4] = {bf_lo(hr.x), bf_hi(hr.x), bf_lo(hr.y), bf_hi(hr.y)};
            float y[4];
#pragma unroll
            for (int e = 0; e < 4; ++e) y[e] = hv[e] + rbf(rbf(o[e]) + bv[e]);
            *(u32x2*)(p.out + (size_t)(32 * s + tok) * DM + 32 * ctile + 8 * w + 4 * hh) = (u32x2){pk_bf(y[0], y[1]), pk_bf(y[2], y[3])};
            if (w == 0 && ctile == 0 && s >= 62) STAMP(300 + s, 5);
        }
    }
}

constexpr int L_F = 0;
constexpr int L_R = 14336;
constexpr int L_MX = L_R + 1792;
constexpr int L_R2 = L_MX + 128;

constexpr unsigned POLL_CAP = 1u << 14;
#define NOCOMM 0
DI u32x4 poll16(rsrc_t r, int vo, int so, unsigned* ctrl, unsigned& cap, unsigned code = 1u, unsigned* np = nullptr) {
    u32x4 v;
    unsigned sp = 0;
    bool bad;
    do {
        asm volatile("" ::: "memory");
        v = LD16(r, vo, so, SC1);
        bad = __any(max(max(v.x, v.y), max(v.z, v.w)) == 0xffffffffu);
    } while (bad && ++sp < cap);
    if (np) *np = sp;
    if (bad) { st_agent(ctrl + CT_ABORT, 1u); cap = 1u; }
    return v;
}
DI float fmax_raw(float a, float b) { float r; asm("v_max_f32 %0, %1, %2" : "=v"(r) : "v"(a), "v"(b)); return r; }
DI float fmax3_raw(float a, float b, float c) { float r; asm("v_max3_f32 %0, %1, %2, %3" : "=v"(r) : "v"(a), "v"(b), "v"(c)); return r; }

#define DELAY_X 7
#define DELAY_Y 0
#define K_Y 132
#define K_X 232
#define T_START 30
#define D_Y 25
#define D_X 25
DI unsigned later(unsigned a, unsigned b) { return (int)(a - b) >= 0 ? a : b; }
DI unsigned now_ticks() { return (unsigned)__builtin_amdgcn_s_memrealtime(); }
DI void wait_until(unsigned target) {
    unsigned long long t;
    unsigned d;
    asm volatile("s_memrealtime %0\n\ts_waitcnt lgkmcnt(0)" : "=s"(t) :: "memory");
    d = target - (unsigned)t;
    if (d - 1u < 4000u) {
        do {
            asm volatile("s_memrealtime %0\n\ts_waitcnt lgkmcnt(0)" : "=s"(t) :: "memory");
        } while ((int)(target - (unsigned)t) > 0);
    }
}
constexpr int L_TX = L_R2 + 4096;

template <bool BROLE, bool W7>
DI void rec_wave(const Params& p, char* smem, const int c, const int w) {
    const int tid = threadIdx.x, lane = tid & 63;
    const int x = lane & 15, q = lane >> 4, ap = (lane >> 3) & 1, i8 = lane & 7;
    const int h = c >> 2, c4 = c & 3;
    const int u = c >= 28 ? 1 : 0, J = c - 28 * u, g = 8 * u + w;
    unsigned* ctrl = (unsigned*)(p.ws + OFF_CTRL);
    const rsrc_t rhx = mk_rsrc(p.hx, (unsigned)SZ_HX), rkt = mk_rsrc(p.ws + OFF_KT, (unsigned)SZ_KT), rvt = mk_rsrc(p.ws + OFF_VT, (unsigned)SZ_KT);
    const rsrc_t rkm = mk_rsrc(p.ws + OFF_KM, (unsigned)SZ_KM), rvm = mk_rsrc(p.ws + OFF_VM, (unsigned)SZ_KM);
    unsigned cap = POLL_CAP;

    u32x4 wA[14], wB[14];
    auto load_w = [&](u32x4 (&wt)[14], int np) {
        const rsrc_t rw = mk_rsrc(p.ws + OFF_WCT, (unsigned)SZ_WCT);
        const int vo = ((np >> 5) * 56 * 32 + q * 32 + (np & 31)) * 16;
#pragma unroll
        for (int ks = 0; ks < 14; ++ks) wt[ks] = LD16(rw, vo + (ks & 1) * 2048, (ks & ~1) * 2048, 0);
    };
    if (W7 || w != 0) {
        if (!W7) load_w(wA, h * 448 + 112 * c4 + 16 * w + x);
        if (BROLE) load_w(wB, 7168 + g * 448 + 16 * J + x);
    }
    if (!W7 && w == 0) {
        unsigned best0 = ~0u, best1 = ~0u;
        for (int rep = 0; rep < 4; ++rep) {
            const int off = c * 65536 + 16384 + (rep & 1) * 8192 + 8064;
            const unsigned long long t0 = __builtin_amdgcn_s_memtime();
            ST4((unsigned)rep, rhx, 0, off, SC1);
            asm volatile("s_waitcnt vmcnt(0)" ::: "memory");
            unsigned v = __builtin_amdgcn_raw_buffer_load_b32(rhx, 0, off, SC1);
            asm volatile("s_waitcnt vmcnt(0)" : "+v"(v) :: "memory");
            const unsigned dt = (unsigned)(__builtin_amdgcn_s_memtime() - t0);
            if (rep & 1) best1 = min(best1, dt); else best0 = min(best0, dt);
        }
        if (lane == 0) st_agent(ctrl + CT_PROBE(c), 2u | (best1 < best0 ? 1u : 0u));
        load_w(wA, h * 448 + 112 * c4 + 16 * w + x);
        if (BROLE) load_w(wB, 7168 + g * 448 + 16 * J + x);
        unsigned long long pr = 0;
        for (unsigned sp = 0; sp < SPIN_CAP; ++sp) {
            const unsigned v = ld_agent(ctrl + CT_PROBE(lane));
            pr = __ballot((v & 1u) != 0u);
            if (!__any(v == 0u)) break;
            __builtin_amdgcn_s_sleep(4);
        }
        const int cnt = __builtin_popcountll((pr >> (lane & ~7)) & 0xffull);
        pr = __ballot(cnt > 4 || (cnt == 4 && ((pr >> lane) & 1ull)));
        if (lane == 0) { *(LAS unsigned long long*)(smem + L_TX + 8) = pr; }
    }
    if (!W7) {
        const int o = tid >> 3, i = tid & 7;
        *(u32x4*)(smem + L_F + (o * 16 + i) * 16) = *(const u32x4*)(p.mem0 + i * DM + o * 8);
        *(u32x4*)(smem + L_F + (o * 16 + 8 + i) * 16) = (u32x4){0u, 0u, 0u, 0u};
    }
    BAR();
    unsigned long long probe;
    {
        const u32x2 pv = *(LAS u32x2*)(smem + L_TX + 8);
        probe = ((unsigned long long)(unsigned)__builtin_amdgcn_readfirstlane(pv.y) << 32) | (unsigned)__builtin_amdgcn_readfirstlane(pv.x);
    }
    {
        const u32x4 ones = {~0u, ~0u, ~0u, ~0u};
        const int so = c * 65536 + (int)(((probe >> c) & 1ull) << 13);
#pragma unroll
        for (int i = 0; i < 4; ++i) ST16(ones, rhx, i * 16384 + tid * 16, so, SC1);
        asm volatile("s_waitcnt vmcnt(0)" ::: "memory");
        BAR();
        if (!W7 && w == 0) {
            if (lane == 0) add_agent(ctrl + CT_START(c & 7), 1u);
            wait_shards(ctrl, CT_KV(0, 0), KV_PER_SHARD, 0);
            wait_shards(ctrl, CT_KV(1, 0), KV_PER_SHARD, 0);
            for (unsigned sp = 0; sp < SPIN_CAP; ++sp) {
                if (!__any(ld_agent(ctrl + CT_START(lane & 7)) < 8u)) break;
                __builtin_amdgcn_s_sleep(4);
            }
        }
        BAR();
    }
    STAMP(c, w);
    auto phys = [](int lo) { return ((lo >> 13) << 14) + (lo & 8191); };
    const int vo_x0 = phys(tid * 16), vo_x1 = phys(7168 + tid * 16);
    const int lo_f = L_F + ((tid >> 3) * 16 + (tid & 7)) * 16;
    const int vo_yl = phys(16384 + (g * 4 + 2 * ap + (q >> 1)) * 256 + i8 * 32 + (q & 1) * 16);
    const int vo_ys = phys(16384 + c * 256 + lane * 4);
    const int vo_xs = phys(u * 7168 + J * 256 + lane * 4);
    const int vo_ts = phys(14336 + c * 16);
    const int vo_k = (q * 32 + 8 * c4 + i8) * 16;
    const int vo_v = ((q & 1) * 32 + 16 * (J & 1) + x) * 16 + 8 * (q >> 1);
    const int vo_km = ((14 * c4 + 2 * w + (q >> 1)) * 8 + i8) * 16 + (q & 1) * 8;
    const int vo_vm = (16 * J + x) * 16 + (q & 1) * 8;
    const int scol = 112 * c4 + 16 * w + 4 * q;
    const int lo_s = L_F + ((scol >> 3) * 16 + x) * 16 + (scol & 4) * 2;
    auto step_off = [&](int s) { return s * 65536 + (int)(((probe >> s) & 1ull) << 13); };

    u32x2 v1 = {0u, 0u}, v2 = {0u, 0u}, vn1 = {0u, 0u}, vn2 = {0u, 0u};
    auto v_load = [&](int s) {
        const int so = ((s * 16 + g) * 14 + (J >> 1)) * 2048;
        vn1 = LD8(rvt, vo_v, so, SC1); vn2 = LD8(rvt, vo_v + 1024, so, SC1);
    };
    auto k_load = [&](int s) {
#pragma unroll
        for (int ks = 0; ks < 14; ++ks) wA[ks] = LD16(rkt, vo_k, (s * 16 + h) * 28672 + ks * 2048, SC1);
    };
    if (BROLE) v_load(0);
    if (W7) k_load(0);
    u32x4 fr[14];
    int kv_ready = 2;
    const unsigned long long m_ap = __ballot(ap != 0);
    const unsigned hm = (q >> 1) ? 0xffff0000u : 0x0000ffffu, m_a = ap ? 0u : hm, m_b = ap ? hm : 0u;
    const unsigned HI = 0xffff0000u;
    const float L2E = LOG2E, NL2E = -LOG2E;
#define SB() __builtin_amdgcn_sched_barrier(0)
    const int vo_t0 = phys(14336);
    unsigned ts_val = 0xffffffffu;
    auto ts_request = [&](int so_s) { asm volatile("" ::: "memory"); ts_val = __builtin_amdgcn_raw_buffer_load_b32(rhx, vo_t0, so_s, SC1); };
    auto ts_finish = [&](int s, int so_s) {
        if (__builtin_expect(NOCOMM == 0 && __builtin_amdgcn_readfirstlane(ts_val) == 0xffffffffu, 0)) {
            unsigned sp = 0;
            do { ts_request(so_s); } while (__builtin_amdgcn_readfirstlane(ts_val) == 0xffffffffu && ++sp < cap);
            if (sp >= cap) { st_agent(ctrl + CT_ABORT, 1u); cap = 1u; }
        }
        if (lane == 0) *(LAS unsigned*)(smem + L_TX + (s & 1) * 4) = ts_val;
    };
    auto publish_t = [&](int so_s) { if (lane == 0) ST4(min(now_ticks(), 0xfffffffeu), rhx, vo_ts, so_s, SC1); };
    unsigned x_tries = 0;
    auto gather_x = [&](int so_s) {
        auto pass = [&]() {
            asm volatile("" ::: "memory");
            const u32x4 a0 = LD16(rhx, vo_x0, so_s, SC1), a1 = LD16(rhx, vo_x1, so_s, SC1);
            *(u32x4*)(smem + lo_f) = a0;
            *(u32x4*)(smem + lo_f + 128) = a1;
            return NOCOMM == 0 && __any(max(max(max(a0.x, a0.y), max(a0.z, a0.w)), max(max(a1.x, a1.y), max(a1.z, a1.w))) == 0xffffffffu);
        };
        x_tries = 0;
        if (__builtin_expect(pass(), 0)) {
            bool bad = true;
            while (bad && ++x_tries < cap) bad = pass();
            if (bad) { st_agent(ctrl + CT_ABORT, 1u); cap = 1u; }
        }
    };
    auto a_mfma = [&]() {
#pragma unroll
        for (int ks = 0; ks < 4; ++ks) fr[ks] = *(const u32x4*)(smem + L_F + ks * 1024 + lane * 16);
        SB();
        f32x4 acc;
        asm volatile("v_mfma_f32_16x16x32_bf16 %0, %1, %2, 0" : "=&v"(acc) : "v"(wA[0]), "v"(fr[0]));
        fr[4] = *(const u32x4*)(smem + L_F + 4 * 1024 + lane * 16);
        SB();
#pragma unroll
        for (int ks = 1; ks < 14; ++ks) {
            asm volatile("v_mfma_f32_16x16x32_bf16 %0, %1, %2, %0" : "+v"(acc) : "v"(wA[ks]), "v"(fr[ks]));
            if (ks + 4 < 14) fr[ks + 4] = *(const u32x4*)(smem + L_F + (ks + 4) * 1024 + lane * 16);
            SB();
        }
        float a0, a1, a2, a3;
        asm volatile("s_nop 7\n\t"
                     "v_add_f32_dpp %0, %4, %4 row_ror:8 row_mask:0xf bank_mask:0xf\n\tv_add_f32_dpp %1, %5, %5 row_ror:8 row_mask:0xf bank_mask:0xf\n\t"
                     "v_add_f32_dpp %2, %6, %6 row_ror:8 row_mask:0xf bank_mask:0xf\n\tv_add_f32_dpp %3, %7, %7 row_ror:8 row_mask:0xf bank_mask:0xf"
                     : "=&v"(a0), "=&v"(a1), "=&v"(a2), "=&v"(a3) : "v"(acc[0]), "v"(acc[1]), "v"(acc[2]), "v"(acc[3]));
        return (u32x2){pk_bf(a0, a1), pk_bf(a2, a3)};
    };
    auto u_part = [&]() {
        f32x4 ua = {0.f, 0.f, 0.f, 0.f};
#pragma unroll
        for (int ks = 0; ks < 14; ++ks) ua = mfma16(fr[ks], wB[ks], ua);
#pragma unroll
        for (int r = 0; r < 4; ++r) ua[r] = xsum32(ua[r]);
        return (u32x2){pk_bf(ua[0], ua[1]), pk_bf(ua[2], ua[3])};
    };

    int so_s = step_off(0);
    if (W7) {
        if (c == 0 && lane == 0) ST4(min(now_ticks() - T_START, 0xfffffffeu), rhx, vo_ts, so_s, SC1);
        ts_request(so_s);
    }
    for (int s = 0; s < NSEG - 1; ++s) {
        u32x2 tb;
        if (BROLE) { v1 = vn1; v2 = vn2; }
        BAR();
        RSTAMP(1, s * 16 + 0);
        if (BROLE) v_load(s + 1);
        if (!W7) {
            const u32x2 sop = *(const u32x2*)(smem + lo_s);
            tb = a_mfma();
            f32x4 st;
            float s0, s1, s2, s3;
            asm volatile("s_nop 1\n\tv_mfma_f32_16x16x16_bf16 %0, %1, %2, 0" : "=&v"(st) : "v"(tb), "v"(sop));
            asm volatile("s_nop 7\n\t"
                         "v_add_f32_dpp %0, %4, %4 row_ror:8 row_mask:0xf bank_mask:0xf\n\tv_add_f32_dpp %1, %5, %5 row_ror:8 row_mask:0xf bank_mask:0xf\n\t"
                         "v_add_f32_dpp %2, %6, %6 row_ror:8 row_mask:0xf bank_mask:0xf\n\tv_add_f32_dpp %3, %7, %7 row_ror:8 row_mask:0xf bank_mask:0xf"
                         : "=&v"(s0), "=&v"(s1), "=&v"(s2), "=&v"(s3) : "v"(st[0]), "v"(st[1]), "v"(st[2]), "v"(st[3]));
            RSTAMP(1, s * 16 + 1);
            if (x < 8 && q < 2) *(f32x4*)(smem + L_R + (w * 16 + 2 * x + q) * 16) = (f32x4){s0, s1, s2, s3};
        } else {
            tb = a_mfma();
            if (x < 8 && q < 2) *(u32x2*)(smem + L_MX + (2 * x + q) * 8) = tb;
            SB();
            ts_finish(s, so_s);
        }
        BAR();
        RSTAMP(1, s * 16 + 2);
        const unsigned t_b2 = now_ticks();
        if (!W7 && w == 0) {
            const int chunk = lane >> 2, part = lane & 3;
            f32x2 t[7];
#pragma unroll
            for (int i = 0; i < 7; ++i) t[i] = *(const f32x2*)(smem + L_R + (i * 16 + chunk) * 16 + (part & 1) * 8);
            unsigned mxv = *(const unsigned*)(smem + L_MX + chunk * 8 + (part & 1) * 4);
            asm volatile("" : "+v"(mxv));
            const f32x2 sum = ((t[0] + t[1]) + (t[2] + t[3])) + ((t[4] + t[5]) + t[6]);
            unsigned mmv = pk_bf(sum[0], sum[1]);
            asm volatile("" : "+v"(mmv));
            ST4(min(part < 2 ? mxv : mmv, 0xfffffffeu), rhx, vo_ys, so_s, SC1);
            RSTAMP(0, s * 16 + 3); RVAL(0, s * 16 + 14, (unsigned)__builtin_amdgcn_s_memrealtime());
        }
        SB();
        const unsigned t_prev = __builtin_amdgcn_readfirstlane(*(LAS unsigned*)(smem + L_TX + (s & 1) * 4));
        const int so_n = step_off(s + 1);
        if (!W7 && s > 0 && w == 3 && tid_opaque() == 192) add_agent(ctrl + CT_M(s - 1, c & 7), 1u);
        if (!W7 && x < 8) ST8(tb, rkm, vo_km, (s * 16 + h) * 7168, SC1);
        if (W7) k_load(s + 1);
        unsigned t_x = t_prev + K_X;
        if (BROLE) {
            RSTAMP(1, s * 16 + 4);
            wait_until(later(t_prev + K_Y, t_b2 + D_Y));
            RSTAMP(1, s * 16 + 5);
            asm volatile("" ::: "memory");
            u32x4 y = LD16(rhx, vo_yl, so_s, SC1);
            SB();
            const u32x2 ub = u_part();
            SB();
            unsigned sp = 0;
            float o0, o1_, o2_, o3;
            auto pass = [&]() {
                unsigned long long bad;
                float x0, x1, x2, x3, sm, p0, p1, p2, p3, m, t;
                asm volatile(
                    "v_lshlrev_b32 %[p0], 16, %[y2]\n\tv_and_b32 %[p1], %[hi], %[y2]\n\tv_lshlrev_b32 %[p2], 16, %[y3]\n\tv_and_b32 %[p3], %[hi], %[y3]\n\t"
                    "v_lshlrev_b32 %[x0], 16, %[y0]\n\tv_and_b32 %[x1], %[hi], %[y0]\n\t"
                    "v_permlane32_swap_b32 %[p0], %[p1]\n\tv_permlane32_swap_b32 %[p2], %[p3]\n\t"
                    "v_lshlrev_b32 %[x2], 16, %[y1]\n\tv_and_b32 %[x3], %[hi], %[y1]\n\t"
                    "v_add_f32 %[p0], %[p0], %[p1]\n\tv_add_f32 %[p2], %[p2], %[p3]\n\t"
                    "v_max3_u32 %[t], %[y0], %[y1], %[y2]\n\t"
                    "v_cndmask_b32 %[p1], %[p2], %[p0], %[ap]\n\tv_cndmask_b32 %[p3], %[p0], %[p2], %[ap]\n\t"
                    "v_max3_f32 %[m], %[x0], %[x1], %[x2]\n\t"
                    "v_max_u32 %[t], %[t], %[y3]\n\t"
                    "v_add_f32_dpp %[sm], %[p1], %[p3] row_ror:8 row_mask:0xf bank_mask:0xf\n\t"
                    "v_cmp_eq_u32 %[bad], -1, %[t]\n\t"
                    "v_max3_f32 %[m], %[m], %[x3], %[sm]\n\t"
                    "s_nop 1\n\t"
                    "v_max_f32_dpp %[m], %[m], %[m] row_ror:8 row_mask:0xf bank_mask:0xf\n\t"
                    "v_mov_b32 %[t], %[m]\n\ts_nop 1\n\tv_permlane16_swap_b32 %[m], %[t]\n\tv_max_f32 %[m], %[m], %[t]\n\t"
                    "v_mov_b32 %[t], %[m]\n\ts_nop 1\n\tv_permlane32_swap_b32 %[m], %[t]\n\tv_max_f32 %[m], %[m], %[t]\n\t"
                    "v_mul_f32 %[m], %[nl2e], %[m]\n\t"
                    "v_fma_f32 %[sm], %[sm], %[l2e], %[m]\n\tv_fma_f32 %[x0], %[x0], %[l2e], %[m]\n\tv_fma_f32 %[x1], %[x1], %[l2e], %[m]\n\t"
                    "v_fma_f32 %[x2], %[x2], %[l2e], %[m]\n\tv_fma_f32 %[x3], %[x3], %[l2e], %[m]\n\t"
                    "v_exp_f32 %[sm], %[sm]\n\tv_exp_f32 %[x0], %[x0]\n\tv_exp_f32 %[x1], %[x1]\n\tv_exp_f32 %[x2], %[x2]\n\tv_exp_f32 %[x3], %[x3]\n\ts_nop 0"
                    : [x0] "=&v"(x0), [x1] "=&v"(x1), [x2] "=&v"(x2), [x3] "=&v"(x3), [sm] "=&v"(sm), [p0] "=&v"(p0), [p1] "=&v"(p1), [p2] "=&v"(p2), [p3] "=&v"(p3),
                      [m] "=&v"(m), [t] "=&v"(t), [bad] "=&s"(bad)
                    : [y0] "v"(y.x), [y1] "v"(y.y), [y2] "v"(y.z), [y3] "v"(y.w), [hi] "s"(HI), [ap] "s"(m_ap), [l2e] "s"(L2E), [nl2e] "s"(NL2E));
                const unsigned tm = pk_bf(sm, sm);
                const u32x2 pb = {pk_bf(x0, x1), pk_bf(x2, x3)};
                const u32x2 pm = {tm & m_a, tm & m_b};
                f32x4 om, oa, ob;
                asm volatile("s_nop 1\n\tv_mfma_f32_16x16x16_bf16 %0, %3, %4, 0\n\tv_mfma_f32_16x16x16_bf16 %1, %5, %7, %0\n\tv_mfma_f32_16x16x16_bf16 %2, %6, %7, %0"
                             : "=&v"(om), "=&v"(oa), "=&v"(ob) : "v"(ub), "v"(pm), "v"(v1), "v"(v2), "v"(pb));
                float l, l2;
                asm volatile(
                    "v_add_f32 %[l], %[x0], %[x1]\n\tv_add_f32 %[l2], %[x2], %[x3]\n\tv_add_f32 %[l], %[l], %[sm]\n\tv_add_f32 %[l], %[l], %[l2]\n\ts_nop 1\n\t"
                    "v_add_f32_dpp %[l], %[l], %[l] row_ror:8 row_mask:0xf bank_mask:0xf\n\t"
                    "v_mov_b32 %[l2], %[l]\n\ts_nop 1\n\tv_permlane16_swap_b32 %[l], %[l2]\n\tv_add_f32 %[l], %[l], %[l2]\n\t"
                    "v_mov_b32 %[l2], %[l]\n\ts_nop 1\n\tv_permlane32_swap_b32 %[l], %[l2]\n\tv_add_f32 %[l], %[l], %[l2]\n\t"
                    "v_rcp_f32 %[l], %[l]\n\t"
                    "v_add_f32_dpp %[r0], %[b0], %[a0] row_ror:8 row_mask:0xf bank_mask:0xf\n\tv_add_f32_dpp %[r1], %[b1], %[a1] row_ror:8 row_mask:0xf bank_mask:0xf\n\t"
                    "v_add_f32_dpp %[r2], %[b2], %[a2] row_ror:8 row_mask:0xf bank_mask:0xf\n\tv_add_f32_dpp %[r3], %[b3], %[a3] row_ror:8 row_mask:0xf bank_mask:0xf\n\t"
                    "v_mul_f32 %[r0], %[r0], %[l]\n\tv_mul_f32 %[r1], %[r1], %[l]\n\tv_mul_f32 %[r2], %[r2], %[l]\n\tv_mul_f32 %[r3], %[r3], %[l]"
                    : [l] "=&v"(l), [l2] "=&v"(l2), [r0] "=&v"(o0), [r1] "=&v"(o1_), [r2] "=&v"(o2_), [r3] "=&v"(o3)
                    : [x0] "v"(x0), [x1] "v"(x1), [x2] "v"(x2), [x3] "v"(x3), [sm] "v"(sm),
                      [a0] "v"(oa[0]), [a1] "v"(oa[1]), [a2] "v"(oa[2]), [a3] "v"(oa[3]), [b0] "v"(ob[0]), [b1] "v"(ob[1]), [b2] "v"(ob[2]), [b3] "v"(ob[3]));
                return NOCOMM == 0 && bad != 0;
            };
            if (__builtin_expect(pass(), 0)) {
                bool bad = true;
                while (bad && ++sp < cap) { asm volatile("" ::: "memory"); y = LD16(rhx, vo_yl, so_s, SC1); bad = pass(); }
                if (bad) { st_agent(ctrl + CT_ABORT, 1u); cap = 1u; }
            }
            const f32x4 o = {o0, o1_, o2_, o3};
            RSTAMP(1, s * 16 + 6);
            if (x < 8) *(f32x4*)(smem + L_R2 + ((w * 8 + x) * 4 + q) * 16) = o;
            BAR();
            RSTAMP(1, s * 16 + 7);
            t_x = later(t_x, now_ticks() + D_X);
            if (W7) {
                const int hf = lane >> 5, i = (lane >> 2) & 7, d = lane & 3;
                f32x2 t[8];
#pragma unroll
                for (int k = 0; k < 8; ++k) t[k] = *(const f32x2*)(smem + L_R2 + ((k * 8 + i) * 4 + 2 * hf + (d >> 1)) * 16 + (d & 1) * 8);
                const f32x2 sum = ((t[0] + t[1]) + (t[2] + t[3])) + ((t[4] + t[5]) + (t[6] + t[7]));
                ST4(min(pk_bf(sum[0], sum[1]), 0xfffffffeu), rhx, vo_xs, so_n, SC1);
                if (c == 0) publish_t(so_n);
                RSTAMP(7, s * 16 + 8); RVAL(7, s * 16 + 15, (unsigned)__builtin_amdgcn_s_memrealtime());
            }
            SB();
            if (q < 2) ST8(ub, rvm, vo_vm, (s * 16 + g) * 7168, SC1);
            RVAL(1, s * 16 + 11, sp + 1);
        }
        if (!BROLE) {
            wait_until(later(t_prev + K_Y, t_b2 + D_Y));
            (void)poll16(rhx, vo_yl, so_s, ctrl, cap);
            t_x = later(t_x, now_ticks() + 30 + D_X);
        }
        if (W7) {
            if (s + 2 < NSEG && s + 2 >= kv_ready) {
                for (unsigned sp = 0; sp < SPIN_CAP; ++sp) {
                    const int sg = s + 2 + (lane >> 3);
                    const unsigned long long rdy = __ballot(sg >= NSEG || ld_agent(ctrl + CT_KV(sg & 63, lane & 7)) >= KV_PER_SHARD);
                    kv_ready = s + 2 + ((rdy == ~0ull ? 64 : __builtin_ctzll(~rdy)) >> 3);
                    if (s + 2 < kv_ready) break;
                    __builtin_amdgcn_s_sleep(4);
                    if (sp + 1 == SPIN_CAP) st_agent(ctrl + CT_ABORT, 1u);
                }
            }
            wait_until(t_x);
            ts_request(so_n);
        } else {
            wait_until(t_x);
            RSTAMP(1, s * 16 + 9);
            gather_x(so_n);
            RVAL(1, s * 16 + 12, x_tries + 1);
            RSTAMP(1, s * 16 + 10);
        }
        so_s = so_n;
    }
    {
        const int s = NSEG - 1;
        if (W7) asm volatile("s_waitcnt vmcnt(0)" ::: "memory");
        BAR();
        if (!W7 && tid_opaque() == 0) add_agent(ctrl + CT_M(s - 1, c & 7), 1u);
        if (!W7) {
            const u32x2 tb = a_mfma();
            if (x < 8) ST8(tb, rkm, vo_km, (s * 16 + h) * 7168, SC1);
        } else {
#pragma unroll
            for (int ks = 0; ks < 14; ++ks) fr[ks] = *(const u32x4*)(smem + L_F + ks * 1024 + lane * 16);
        }
        if (BROLE) { const u32x2 ub = u_part(); if (q < 2) ST8(ub, rvm, vo_vm, (s * 16 + g) * 7168, SC1); }
        asm volatile("s_waitcnt vmcnt(0)" ::: "memory");
        BAR();
        if (!W7 && tid_opaque() == 0) add_agent(ctrl + CT_M(s, c & 7), 1u);
    }
}

DI void rec_role(const Params& p, char* smem, int c) {
    const int w = __builtin_amdgcn_readfirstlane(threadIdx.x >> 6);
    if (c < 56) { if (w < 7) rec_wave<true, false>(p, smem, c, w); else rec_wave<true, true>(p, smem, c, w); }
    else        { if (w < 7) rec_wave<false, false>(p, smem, c, w); else rec_wave<false, true>(p, smem, c, w); }
}

constexpr int LA_X = 0;
constexpr int LA_SM = 28672;
constexpr int LA_PX = LA_SM + 16384;
constexpr int LA_PM = LA_PX + 8192;
constexpr int LA_RED = LA_PM + 2048;
constexpr int LA_BC = LA_RED + 1024;

DI void attn_item(const Params& p, char* smem, int s, int hg) {
    const int tid = threadIdx.x, lane = tid & 63, w = __builtin_amdgcn_readfirstlane(tid >> 6);
    const int tok = lane & 31, hh = lane >> 5;
    unsigned* ctrl = (unsigned*)(p.ws + OFF_CTRL);
    const rsrc_t rkt = mk_rsrc(p.ws + OFF_KT, (unsigned)SZ_KT), rvt = mk_rsrc(p.ws + OFF_VT, (unsigned)SZ_KT);
    const rsrc_t rkm = mk_rsrc(p.ws + OFF_KM, (unsigned)SZ_KM), rvm = mk_rsrc(p.ws + OFF_VM, (unsigned)SZ_KM);
    const rsrc_t rop = mk_rsrc(p.ws + OFF_OP, (unsigned)SZ_OP);
    const rsrc_t rhd = mk_rsrc(p.ws + OFF_HD, (unsigned)(64 * SZ_SEGF)), rhn = mk_rsrc(p.ws + OFF_HNF, (unsigned)(64 * SZ_SEGF));
    {
        const char* src = p.ws + OFF_XNF + (size_t)s * SZ_SEGF;
#pragma unroll
        for (int i = 0; i < 4; ++i) { const int k = w + 8 * i; if (k < 28) GLDS(src + k * 1024 + lane * 16, smem + LA_X + k * 1024, 0); }
    }
    if (w == 0) wait_shards(ctrl, CT_KV(s, 0), KV_PER_SHARD, 2);
    asm volatile("s_waitcnt vmcnt(0)" ::: "memory");
    BAR();
    const int nt = w < 6 ? 2 : 1;
    f32x16 sx;
#pragma unroll
    for (int i = 0; i < 16; ++i) sx[i] = 0.f;
    if (w < 4) {
        u32x4 kf[28];
#pragma unroll
        for (int ks = 0; ks < 28; ++ks) kf[ks] = LD16(rkt, lane * 16, (s * 16 + 4 * hg + w) * 28672 + ks * 1024, SC1);
#pragma unroll
        for (int ks = 0; ks < 28; ++ks) sx = mfma32(kf[ks], *(const u32x4*)(smem + LA_X + ks * 1024 + lane * 16), sx);
    }
    u32x4 vf[2][8];
#pragma unroll
    for (int t = 0; t < 2; ++t)
        if (t < nt) {
#pragma unroll
            for (int k = 0; k < 8; ++k) vf[t][k] = LD16(rvt, lane * 16, (((s * 16 + 4 * hg + (k >> 1)) * 14 + w + 8 * t) * 2 + (k & 1)) * 1024, SC1);
        }
    if (w == 4) wait_shards(ctrl, CT_M(s, 0), M_PER_SHARD, s < 60 ? 4 : 1);
    BAR();
    if (w == 0 && s == 63) STAMP(400 + hg, 10);
    u32x2 vm[2][4];
#pragma unroll
    for (int t = 0; t < 2; ++t)
        if (t < nt) {
#pragma unroll
            for (int k = 0; k < 4; ++k) vm[t][k] = LD8(rvm, (32 * (w + 8 * t) + tok) * 16 + hh * 8, (s * 16 + 4 * hg + k) * 7168, SC1);
        }
    if (w >= 4) {
        const int kq = w - 4;
        u32x4 mf[7];
#pragma unroll
        for (int i = 0; i < 7; ++i) mf[i] = LD16(rkm, (hh * 8 + (tok & 7)) * 16 + (tok >> 3) * 7168, (s * 16 + 4 * hg) * 7168 + (7 * kq + i) * 256, SC1);
        f32x16 sm;
#pragma unroll
        for (int i = 0; i < 16; ++i) sm[i] = 0.f;
#pragma unroll
        for (int i = 0; i < 7; ++i) sm = mfma32(mf[i], *(const u32x4*)(smem + LA_X + (7 * kq + i) * 1024 + lane * 16), sm);
#pragma unroll
        for (int gq = 0; gq < 4; ++gq) *(f32x4*)(smem + LA_SM + ((kq * 4 + gq) * 64 + lane) * 16) = (f32x4){sm[4 * gq], sm[4 * gq + 1], sm[4 * gq + 2], sm[4 * gq + 3]};
    }
    BAR();
    if (w < 4) {
        f32x4 m4 = *(const f32x4*)(smem + LA_SM + ((0 * 4 + w) * 64 + lane) * 16);
#pragma unroll
        for (int kq = 1; kq < 4; ++kq) m4 += *(const f32x4*)(smem + LA_SM + ((kq * 4 + w) * 64 + lane) * 16);
        float mx = fmaxf(fmaxf(m4[0], m4[1]), fmaxf(m4[2], m4[3]));
#pragma unroll
        for (int i = 0; i < 16; ++i) {
            const int key = (i & 3) + 8 * (i >> 2) + 4 * hh;
            sx[i] = key <= tok ? sx[i] : -1e30f;
            mx = fmaxf(mx, sx[i]);
        }
        mx = xmax32(mx);
        const float mc = mx * LOG2E;
        float l = 0.f;
#pragma unroll
        for (int i = 0; i < 16; ++i) { sx[i] = __builtin_amdgcn_exp2f(sx[i] * LOG2E - mc); l += sx[i]; }
#pragma unroll
        for (int i = 0; i < 4; ++i) { m4[i] = __builtin_amdgcn_exp2f(m4[i] * LOG2E - mc); l += m4[i]; }
        l = xsum32(l);
        const float inv = frcp(l);
#pragma unroll
        for (int k = 0; k < 2; ++k) {
            u32x4 v = {pk_bf(sx[8 * k] * inv, sx[8 * k + 1] * inv), pk_bf(sx[8 * k + 2] * inv, sx[8 * k + 3] * inv),
                       pk_bf(sx[8 * k + 4] * inv, sx[8 * k + 5] * inv), pk_bf(sx[8 * k + 6] * inv, sx[8 * k + 7] * inv)};
            *(u32x4*)(smem + LA_PX + ((w * 2 + k) * 64 + lane) * 16) = v;
        }
        *(u32x2*)(smem + LA_PM + (w * 64 + lane) * 8) = (u32x2){pk_bf(m4[0] * inv, m4[1] * inv), pk_bf(m4[2] * inv, m4[3] * inv)};
    }
    BAR();
#pragma unroll
    for (int t = 0; t < 2; ++t)
        if (t < nt) {
            f32x16 o;
#pragma unroll
            for (int i = 0; i < 16; ++i) o[i] = 0.f;
#pragma unroll
            for (int k = 0; k < 8; ++k) o = mfma32(vf[t][k], *(const u32x4*)(smem + LA_PX + (k * 64 + lane) * 16), o);
#pragma unroll
            for (int pp = 0; pp < 2; ++pp) {
                const u32x2 pa = *(const u32x2*)(smem + LA_PM + ((2 * pp) * 64 + lane) * 8), pb = *(const u32x2*)(smem + LA_PM + ((2 * pp + 1) * 64 + lane) * 8);
                o = mfma32((u32x4){vm[t][2 * pp].x, vm[t][2 * pp].y, vm[t][2 * pp + 1].x, vm[t][2 * pp + 1].y}, (u32x4){pa.x, pa.y, pb.x, pb.y}, o);
            }
#pragma unroll
            for (int gq = 0; gq < 4; ++gq) {
                f32x4 v = {o[4 * gq], o[4 * gq + 1], o[4 * gq + 2], o[4 * gq + 3]};
                ST16(__builtin_bit_cast(u32x4, v), rop, lane * 16 + gq * 1024, ((s * 4 + hg) * 14 + w + 8 * t) * 4096, SC1);
            }
        }
    if (w == 0 && s == 63) STAMP(400 + hg, 11);
    asm volatile("s_waitcnt vmcnt(0)" ::: "memory");
    BAR();
    if (w == 0 && s == 63) STAMP(400 + hg, 12);
    if (w == 0) {
        if (lane == 0) add_agent(ctrl + CT_A(s), 1u);
        wait_count(ctrl, CT_A(s), 4u, 0);
    }
    BAR();
    {
        const int t8 = tid & 7, pr = tid >> 3, tk = 8 * hg + t8;
        const int cti = pr >> 2, k = (pr >> 1) & 1, h2 = pr & 1;
        const bool act = tid < 448;
        float hv[8];
        float ssum = 0.f;
        if (act) {
            const int vo = ((cti * 4 + 2 * k) * 64 + h2 * 32 + tk) * 16;
            f32x4 pa[4], pb[4];
#pragma unroll
            for (int g4 = 0; g4 < 4; ++g4) {
                pa[g4] = __builtin_bit_cast(f32x4, LD16(rop, vo, (s * 4 + g4) * 57344, SC1));
                pb[g4] = __builtin_bit_cast(f32x4, LD16(rop, vo + 1024, (s * 4 + g4) * 57344, SC1));
            }
            const uint16_t* xp = p.x + (size_t)(32 * s + tk) * DM + 32 * cti + 16 * k + 4 * h2;
            const u32x2 xa = *(const u32x2*)xp, xb = *(const u32x2*)(xp + 8);
            const f32x4 a = ((pa[0] + pa[1]) + pa[2]) + pa[3], bq = ((pb[0] + pb[1]) + pb[2]) + pb[3];
            const float xv[8] = {bf_lo(xa.x), bf_hi(xa.x), bf_lo(xa.y), bf_hi(xa.y), bf_lo(xb.x), bf_hi(xb.x), bf_lo(xb.y), bf_hi(xb.y)};
#pragma unroll
            for (int e = 0; e < 4; ++e) { hv[e] = rbf(xv[e] + rbf(a[e])); hv[4 + e] = rbf(xv[4 + e] + rbf(bq[e])); }
#pragma unroll
            for (int e = 0; e < 8; ++e) ssum += hv[e];
        } else {
#pragma unroll
            for (int e = 0; e < 8; ++e) hv[e] = 0.f;
        }
        float* red = (float*)(smem + LA_RED);
        ssum += dpp_ror8(ssum); ssum = xsum32(xsum16(ssum));
        if (lane < 8) red[w * 8 + lane] = ssum;
        BAR();
        float mean = 0.f;
#pragma unroll
        for (int q8 = 0; q8 < 7; ++q8) mean += red[q8 * 8 + t8];
        mean *= (1.0f / DM);
        float s2 = 0.f;
        if (act) {
#pragma unroll
            for (int e = 0; e < 8; ++e) { const float d = hv[e] - mean; s2 += d * d; }
        }
        s2 += dpp_ror8(s2); s2 = xsum32(xsum16(s2));
        if (lane < 8) red[64 + w * 8 + lane] = s2;
        BAR();
        float var = 0.f;
#pragma unroll
        for (int q8 = 0; q8 < 7; ++q8) var += red[64 + q8 * 8 + t8];
        var *= (1.0f / DM);
        const float mb = rbf(mean), den = rbf(sqrtf(rbf(rbf(var) + 1e-5f)));
        if (act) {
            float y[8];
            const float rden = frcp(den);
#pragma unroll
            for (int e = 0; e < 8; ++e) y[e] = fdiv(rbf(hv[e] - mb), rden);
            const int vo = ((2 * cti + k) * 64 + h2 * 32 + tk) * 16;
            ST16(((u32x4){pk_bf(hv[0], hv[1]), pk_bf(hv[2], hv[3]), pk_bf(hv[4], hv[5]), pk_bf(hv[6], hv[7])}), rhd, vo, s * (int)SZ_SEGF, SC1);
            ST16(((u32x4){pk_bf(y[0], y[1]), pk_bf(y[2], y[3]), pk_bf(y[4], y[5]), pk_bf(y[6], y[7])}), rhn, vo, s * (int)SZ_SEGF, SC1);
        }
        asm volatile("s_waitcnt vmcnt(0)" ::: "memory");
        BAR();
        if (w == 0 && s == 63) STAMP(404, 10 + hg);
        if (tid_opaque() == 0) add_agent(ctrl + CT_H(s), 1u);
    }
    BAR();
}

constexpr int K2_BC = 122880;
constexpr int K2_LDS = K2_BC + 256;

__global__ void __launch_bounds__(512, 2) k2_main(Params p) {
    extern __shared__ __attribute__((aligned(16))) char smem[];
    const int tid = threadIdx.x;
    unsigned* ctrl = (unsigned*)(p.ws + OFF_CTRL);
    volatile LAS unsigned* bc = (volatile LAS unsigned*)(smem + K2_BC);
    if (tid == 0) {
        const unsigned xc = xcc_id();
        const unsigned r = add_agent(ctrl + CT_TICK(xc), 1u);
        unsigned role;
        if ((xc == 0u || xc == 2u) && r < 32u) role = (xc >> 1) * 32u + r;
        else role = N_REC + add_agent(ctrl + CT_WTICK, 1u);
        asm volatile("s_waitcnt vmcnt(0)" ::: "memory");
        add_agent(ctrl + CT_TOTAL, 1u);
        if (role >= N_WG) {
            unsigned sp = 0;
            while (ld_agent(ctrl + CT_TOTAL) < gridDim.x && ++sp < SPIN_CAP) __builtin_amdgcn_s_sleep(8);
            const unsigned c0 = min(ld_agent(ctrl + CT_TICK(0)), 32u), c2 = min(ld_agent(ctrl + CT_TICK(2)), 32u);
            const unsigned mi = role - N_WG;
            role = mi < 32u - c0 ? c0 + mi : 32u + c2 + (mi - (32u - c0));
        }
        bc[0] = role;
    }
    __syncthreads();
    const int role = __builtin_amdgcn_readfirstlane(bc[0]);
    __syncthreads();
    if (role < N_REC) { rec_role(p, smem, role); return; }
    if (role < N_REC + N_GEMM) { gemm448<0>(p, smem, role - N_REC, 0, 32); __syncthreads(); }
    else if (role < N_REC + N_GEMM + N_F1) { const int i = role - N_REC - N_GEMM; ffn1_role(p, smem, i >> 1, i & 1); return; }
    else if (role < N_REC + N_GEMM + N_F1 + N_F2) { const int i = role - N_REC - N_GEMM - N_F1; ffn2_role(p, smem, i >> 1, i & 1); return; }
    for (;;) {
        if (tid_opaque() == 0) bc[0] = add_agent(ctrl + CT_ATICK, 1u);
        __syncthreads();
        const int item = __builtin_amdgcn_readfirstlane(bc[0]);
        __syncthreads();
        if (item >= 4 * NSEG) break;
        if (tid_opaque() < 64) STAMP(256 + item, 0);
        attn_item(p, smem, item >> 2, item & 3);
        if (tid_opaque() < 64) STAMP(256 + item, 1);
    }
}

extern "C" void kernel_launch(void* const* d_in, const int* in_sizes, int n_in, void* d_out, int out_size, void* d_ws, size_t ws_size, hipStream_t stream) {
    Params p;
    p.x = (const uint16_t*)d_in[0]; p.mem0 = (const uint16_t*)d_in[1];
    p.wq = (const uint16_t*)d_in[2]; p.wk = (const uint16_t*)d_in[3]; p.wv = (const uint16_t*)d_in[4]; p.wo = (const uint16_t*)d_in[5];
    p.w1 = (const uint16_t*)d_in[8]; p.b1 = (const uint16_t*)d_in[9]; p.w2 = (const uint16_t*)d_in[10]; p.b2 = (const uint16_t*)d_in[11];
    p.out = (uint16_t*)d_out; p.ws = (char*)d_ws;
    uintptr_t hx = ((uintptr_t)d_ws + OFF_END + ((size_t)2 << 20) - 1) & ~(uintptr_t)(((size_t)2 << 20) - 1);
    p.hx = (char*)hx;
    (void)hipFuncSetAttribute((const void*)k2_main, hipFuncAttributeMaxDynamicSharedMemorySize, K2_LDS);
    (void)hipFuncSetAttribute((const void*)k1_prep, hipFuncAttributeMaxDynamicSharedMemorySize, K1_LDS);
    hipLaunchKernelGGL(k1_prep, dim3(K1_GRID), dim3(256), K1_LDS, stream, p);
    hipLaunchKernelGGL(k2_main, dim3(N_WG), dim3(512), K2_LDS, stream, p);
}
```

```cpp
#include <hip/hip_runtime.h>
#include <hip/hip_bf16.h>
#include <stdint.h>

typedef __bf16 bf16x8 __attribute__((ext_vector_type(8)));
typedef __bf16 bf16x2 __attribute__((ext_vector_type(2)));
typedef short s16x4 __attribute__((ext_vector_type(4)));
typedef float f32x2 __attribute__((ext_vector_type(2)));
typedef float f32x4 __attribute__((ext_vector_type(4)));
typedef float f32x16 __attribute__((ext_vector_type(16)));
typedef unsigned u32x2 __attribute__((ext_vector_type(2)));
typedef unsigned u32x4 __attribute__((ext_vector_type(4)));
#define LAS __attribute__((address_space(3)))
#define GAS __attribute__((address_space(1)))
#define DI __device__ __forceinline__
typedef __amdgpu_buffer_rsrc_t rsrc_t;

constexpr int DM = 448, NH = 16, DH = 256, SEQ = 2048, NSEG = 64, FF = 1792;

constexpr size_t OFF_CTRL = 0;
constexpr size_t SZ_CTRL = 262144;
constexpr size_t OFF_XNF = OFF_CTRL + SZ_CTRL;
constexpr size_t SZ_SEGF = 28 * 1024;
constexpr size_t OFF_WCT = OFF_XNF + 64 * SZ_SEGF;
constexpr size_t SZ_WCT = (size_t)448 * 56 * 512;
constexpr size_t OFF_KT = OFF_WCT + SZ_WCT;
constexpr size_t SZ_KT = (size_t)64 * 16 * 28672;
constexpr size_t OFF_VT = OFF_KT + SZ_KT;
constexpr size_t OFF_KM = OFF_VT + SZ_KT;
constexpr size_t SZ_KM = (size_t)64 * 16 * 7168;
constexpr size_t OFF_VM = OFF_KM + SZ_KM;
constexpr size_t OFF_OP = OFF_VM + SZ_KM;
constexpr size_t SZ_OP = (size_t)64 * 4 * 57344;
constexpr size_t OFF_HD = OFF_OP + SZ_OP;
constexpr size_t OFF_HNF = OFF_HD + 64 * SZ_SEGF;
constexpr size_t OFF_RF = OFF_HNF + 64 * SZ_SEGF;
constexpr size_t SZ_RF = (size_t)64 * 112 * 1024;
constexpr size_t OFF_END = OFF_RF + SZ_RF;
constexpr size_t SZ_HX = (size_t)4 << 20;

#define CT_TICK(x) (64 * (x))
#define CT_WTICK (64 * 16)
#define CT_TOTAL (64 * 17)
#define CT_ATICK (64 * 18)
#define CT_ABORT (64 * 19)
#define CT_PROBE(c) (57344 + (c) * 32)
#define CT_START(sh) (64 * (22 + (sh)))
#define CT_A(s) (64 * (32 + (s)))
#define CT_H(s) (49152 + (s) * 32)
#define CT_R(s) (49152 + 2048 + (s) * 32)
#define CT_KV(s, sh) (8192 + ((s) * 8 + (sh)) * 32)
#define CT_M(s, sh) (8192 + 16384 + ((s) * 8 + (sh)) * 32)
constexpr unsigned KV_PER_SHARD = 14, M_PER_SHARD = 8;

constexpr int N_REC = 64, N_GEMM = 112, N_F1 = 28, N_F2 = 28, N_WG = 256;
constexpr unsigned SPIN_CAP = 1u << 15;
constexpr float LOG2E = 1.4426950408889634f;

struct Params {
    const uint16_t* x; const uint16_t* mem0; const uint16_t* wq; const uint16_t* wk; const uint16_t* wv; const uint16_t* wo;
    const uint16_t* w1; const uint16_t* b1; const uint16_t* w2; const uint16_t* b2;
    uint16_t* out; char* ws; char* hx;
};

DI float bf_lo(unsigned v) { return __builtin_bit_cast(float, v << 16); }
DI float bf_hi(unsigned v) { return __builtin_bit_cast(float, v & 0xffff0000u); }
DI unsigned pk_bf(float a, float b) { bf16x2 r = {(__bf16)a, (__bf16)b}; return __builtin_bit_cast(unsigned, r); }
DI float rbf(float a) { return bf_lo(pk_bf(a, a)); }
DI float dpp_ror8(float v) { return __builtin_bit_cast(float, __builtin_amdgcn_update_dpp(0, __builtin_bit_cast(int, v), 0x128, 0xf, 0xf, true)); }
DI unsigned fb(float v) { return __builtin_bit_cast(unsigned, v); }
DI float bfl(unsigned v) { return __builtin_bit_cast(float, v); }
DI float xsum32(float v) { auto r = __builtin_amdgcn_permlane32_swap(fb(v), fb(v), false, false); return bfl(r[0]) + bfl(r[1]); }
DI float xsum16(float v) { auto r = __builtin_amdgcn_permlane16_swap(fb(v), fb(v), false, false); return bfl(r[0]) + bfl(r[1]); }
DI float xmax32(float v) { auto r = __builtin_amdgcn_permlane32_swap(fb(v), fb(v), false, false); return fmaxf(bfl(r[0]), bfl(r[1])); }
DI f32x4 mfma16(u32x4 a, u32x4 b, f32x4 c) { return __builtin_amdgcn_mfma_f32_16x16x32_bf16(__builtin_bit_cast(bf16x8, a), __builtin_bit_cast(bf16x8, b), c, 0, 0, 0); }
DI f32x16 mfma32(u32x4 a, u32x4 b, f32x16 c) { return __builtin_amdgcn_mfma_f32_32x32x16_bf16(__builtin_bit_cast(bf16x8, a), __builtin_bit_cast(bf16x8, b), c, 0, 0, 0); }
DI rsrc_t mk_rsrc(const void* p, unsigned bytes) { return __builtin_amdgcn_make_buffer_rsrc((void*)p, 0, bytes, 0x00020000); }
#define LD16(r, vo, so, aux) __builtin_amdgcn_raw_buffer_load_b128((r), (vo), (so), (aux))
#define LD8(r, vo, so, aux) __builtin_amdgcn_raw_buffer_load_b64((r), (vo), (so), (aux))
#define ST16(v, r, vo, so, aux) do { const u32x4 st16_v_ = (v); __builtin_amdgcn_raw_buffer_store_b128(st16_v_, (r), (vo), (so), (aux)); asm volatile("s_nop 1" :: "v"(st16_v_)); } while (0)
#define ST8(v, r, vo, so, aux) __builtin_amdgcn_raw_buffer_store_b64((v), (r), (vo), (so), (aux))
#define ST4(v, r, vo, so, aux) __builtin_amdgcn_raw_buffer_store_b32((v), (r), (vo), (so), (aux))
constexpr int SC1 = 16;
#define BAR() do { asm volatile("s_waitcnt lgkmcnt(0)" ::: "memory"); __builtin_amdgcn_s_barrier(); asm volatile("" ::: "memory"); } while (0)
#define GLDS(src, dst, aux) __builtin_amdgcn_global_load_lds((const GAS void*)(src), (LAS void*)(dst), 16, 0, (aux))
DI unsigned ld_agent(const unsigned* p) { return __hip_atomic_load((const GAS unsigned*)p, __ATOMIC_RELAXED, __HIP_MEMORY_SCOPE_AGENT); }
DI void st_agent(unsigned* p, unsigned v) { __hip_atomic_store((GAS unsigned*)p, v, __ATOMIC_RELAXED, __HIP_MEMORY_SCOPE_AGENT); }
DI unsigned add_agent(unsigned* p, unsigned v) { return __hip_atomic_fetch_add((GAS unsigned*)p, v, __ATOMIC_RELAXED, __HIP_MEMORY_SCOPE_AGENT); }
DI int tid_opaque() { int t = threadIdx.x; asm volatile("" : "+v"(t)); return t; }
DI unsigned xcc_id() { return (unsigned)__builtin_amdgcn_s_getreg(20 | (3 << 11)) & 0xfu; }
DI float fdiv(float a, float rb) { return a * rb; }
DI float frcp(float b) { const float r = __builtin_amdgcn_rcpf(b); return __builtin_fmaf(__builtin_fmaf(-b, r, 1.0f), r, r); }
DI float wave_sum(float v) {
    v += dpp_ror8(v);
    v += __builtin_bit_cast(float, __builtin_amdgcn_update_dpp(0, __builtin_bit_cast(int, v), 0x124, 0xf, 0xf, true));
    v += __builtin_bit_cast(float, __builtin_amdgcn_update_dpp(0, __builtin_bit_cast(int, v), 0x122, 0xf, 0xf, true));
    v += __builtin_bit_cast(float, __builtin_amdgcn_update_dpp(0, __builtin_bit_cast(int, v), 0x121, 0xf, 0xf, true));
    return xsum32(xsum16(v));
}
DI void wait_count(unsigned* ctrl, int word, unsigned target, int slp) {
    for (unsigned sp = 0; sp < SPIN_CAP; ++sp) {
        unsigned v = __builtin_amdgcn_readfirstlane(ld_agent(ctrl + word));
        if (v >= target) return;
        __builtin_amdgcn_s_sleep(2);
        for (int i = 0; i < slp; ++i) __builtin_amdgcn_s_sleep(8);
        if ((sp & 255u) == 255u && __builtin_amdgcn_readfirstlane(ld_agent(ctrl + CT_ABORT))) return;
    }
    st_agent(ctrl + CT_ABORT, 1u);
}

DI void wait_shards(unsigned* ctrl, int word, unsigned target, int slp) {
    const int lane = threadIdx.x & 7;
    for (unsigned sp = 0; sp < SPIN_CAP; ++sp) {
        if (!__any(ld_agent(ctrl + word + lane * 32) < target)) return;
        __builtin_amdgcn_s_sleep(2);
        for (int i = 0; i < slp; ++i) __builtin_amdgcn_s_sleep(8);
        if ((sp & 63u) == 63u && __builtin_amdgcn_readfirstlane(ld_agent(ctrl + CT_ABORT))) return;
    }
    st_agent(ctrl + CT_ABORT, 1u);
}

constexpr int K1_GRID = 512;
constexpr int K1_Q = 2 * 112 * 128;
constexpr int K1_LDS = 2 * K1_Q + 1024;

__global__ void __launch_bounds__(256, 2) k1_prep(Params p) {
    extern __shared__ __attribute__((aligned(16))) char smem[];
    const int b = blockIdx.x, tid = threadIdx.x, lane = tid & 63, w = __builtin_amdgcn_readfirstlane(tid >> 6);
    {
        const int m = (b & 7) + 8 * (b >> 7), tr = (b >> 5) & 3, tc = (b >> 3) & 3, hd = m & 15;
        const bool isA = m < 16;
        const uint16_t* arow = (isA ? p.wk : p.wv) + (size_t)(112 * tr) * 4096 + hd * 256;
        const uint16_t* brow = p.wq + (size_t)(112 * tc) * 4096 + hd * 256;
        const uint16_t* bcol = p.wo + (size_t)hd * 256 * 448 + 112 * tc;
        auto stage = [&](int kq, int slot) {
            char* base = smem + slot * K1_Q;
#pragma unroll
            for (int i = 0; i < 4; ++i) {
                const int j = w + 4 * i;
                if (j < 14) {
                    const int r = 8 * j + (lane >> 3), c = (lane & 7) ^ ((r ^ (r >> 3)) & 7);
                    GLDS(arow + (size_t)r * 4096 + kq * 64 + c * 8, base + j * 1024, 0);
                    if (isA) GLDS(brow + (size_t)r * 4096 + kq * 64 + c * 8, base + 14336 + j * 1024, 0);
                    else { const int ch = 64 * j + lane, e = ch / 14; GLDS(bcol + (size_t)(kq * 64 + e) * 448 + (ch - 14 * e) * 8, base + 14336 + j * 1024, 0); }
                }
            }
        };
        stage(0, 0); stage(1, 1);
        const int row = 4 * b + w;
        u32x4 raw = {0u, 0u, 0u, 0u};
        if (lane < 56) raw = *(const u32x4*)(p.x + (size_t)row * DM + lane * 8);
        if (tid < 32) *(u32x4*)(p.ws + OFF_CTRL + ((size_t)b * 32 + tid) * 16) = (u32x4){0u, 0u, 0u, 0u};
        {
            float v[8];
            v[0] = bf_lo(raw.x); v[1] = bf_hi(raw.x); v[2] = bf_lo(raw.y); v[3] = bf_hi(raw.y);
            v[4] = bf_lo(raw.z); v[5] = bf_hi(raw.z); v[6] = bf_lo(raw.w); v[7] = bf_hi(raw.w);
            float s = 0.f;
#pragma unroll
            for (int e = 0; e < 8; ++e) s += v[e];
            const float mean = wave_sum(s) * (1.0f / DM);
            float s2 = 0.f;
            if (lane < 56) {
#pragma unroll
                for (int e = 0; e < 8; ++e) { const float d = v[e] - mean; s2 += d * d; }
            }
            const float var = wave_sum(s2) * (1.0f / DM);
            const float mb = rbf(mean);
            const float den = rbf(sqrtf(rbf(rbf(var) + 1e-5f)));
            if (lane < 56) {
                float y[8];
                const float rden = frcp(den);
#pragma unroll
                for (int e = 0; e < 8; ++e) y[e] = fdiv(rbf(v[e] - mb), rden);
                const u32x4 o = {pk_bf(y[0], y[1]), pk_bf(y[2], y[3]), pk_bf(y[4], y[5]), pk_bf(y[6], y[7])};
                *(u32x4*)(p.ws + OFF_XNF + (size_t)(row >> 5) * SZ_SEGF + ((lane >> 1) * 64 + (lane & 1) * 32 + (row & 31)) * 16) = o;
            }
        }
        f32x4 acc[2][7];
#pragma unroll
        for (int i = 0; i < 2; ++i)
#pragma unroll
            for (int j = 0; j < 7; ++j) acc[i][j] = (f32x4){0.f, 0.f, 0.f, 0.f};
        const int x = lane & 15, q = lane >> 4;
        const int nst = w < 2 ? 8 : 6;
#pragma unroll
        for (int kq = 0; kq < 4; ++kq) {
            if (kq < 3) { if (nst == 8) asm volatile("s_waitcnt vmcnt(8)" ::: "memory"); else asm volatile("s_waitcnt vmcnt(6)" ::: "memory"); }
            else asm volatile("s_waitcnt vmcnt(0)" ::: "memory");
            BAR();
            const char* As = smem + (kq & 1) * K1_Q; const char* Bs = As + 14336;
            u32x4 bf[2][7], af[2][2];
#pragma unroll
            for (int ks = 0; ks < 2; ++ks) {
#pragma unroll
                for (int j = 0; j < 7; ++j) {
                    if (isA) { const int r = 16 * j + x; bf[ks][j] = *(const u32x4*)(Bs + r * 128 + (((4 * ks + q) ^ ((r ^ (r >> 3)) & 7)) * 16)); }
                    else {
                        const int r0 = 32 * ks + 8 * q + (x >> 2), cb = (16 * j + 4 * (x & 3)) * 2;
                        const s16x4 lo = __builtin_amdgcn_ds_read_tr16_b64_v4i16((LAS s16x4*)(Bs + r0 * 224 + cb));
                        const s16x4 hi = __builtin_amdgcn_ds_read_tr16_b64_v4i16((LAS s16x4*)(Bs + (r0 + 4) * 224 + cb));
                        const u32x2 l2 = __builtin_bit_cast(u32x2, lo), h2 = __builtin_bit_cast(u32x2, hi);
                        bf[ks][j] = (u32x4){l2.x, l2.y, h2.x, h2.y};
                    }
                }
#pragma unroll
                for (int i = 0; i < 2; ++i) { const int r = 16 * (w + 4 * i < 7 ? w + 4 * i : 6) + x; af[ks][i] = *(const u32x4*)(As + r * 128 + (((4 * ks + q) ^ ((r ^ (r >> 3)) & 7)) * 16)); }
            }
            __builtin_amdgcn_sched_barrier(0);
#pragma unroll
            for (int ks = 0; ks < 2; ++ks)
#pragma unroll
                for (int i = 0; i < 2; ++i)
#pragma unroll
                    for (int j = 0; j < 7; ++j) acc[i][j] = mfma16(af[ks][i], bf[ks][j], acc[i][j]);
            if (kq + 2 < 4) { BAR(); stage(kq + 2, kq & 1); }
        }
        const float sc = isA ? 0.0625f : 1.0f;
        BAR();
        char* Ts = smem;
#pragma unroll
        for (int i = 0; i < 2; ++i) {
            const int rt = w + 4 * i;
            if (rt < 7) {
#pragma unroll
                for (int j = 0; j < 7; ++j)
                    *(u32x2*)(Ts + (16 * j + x) * 240 + (16 * rt + 4 * q) * 2) = (u32x2){pk_bf(acc[i][j][0] * sc, acc[i][j][1] * sc), pk_bf(acc[i][j][2] * sc, acc[i][j][3] * sc)};
            }
        }
        BAR();
        const rsrc_t rw = mk_rsrc(p.ws + OFF_WCT, (unsigned)SZ_WCT);
        const int np0 = (isA ? 0 : 7168) + hd * 448 + 112 * tc;
#pragma unroll
        for (int i = 0; i < 7; ++i) {
            const int L = (tid + 256 * i) >> 3;
            if (L < 196) {
                const int ng = L / 14, o = L - 14 * ng, np = np0 + 8 * ng + (lane & 7);
                ST16(*(const u32x4*)(Ts + (8 * ng + (lane & 7)) * 240 + o * 16), rw, (((np >> 5) * 56 + 14 * tr + o) * 32 + (np & 31)) * 16, 0, SC1);
            }
        }
    }
}

DI void proj_role(const Params& p, char* smem, int slice) {
    constexpr int npair = NSEG / 2;
    const int tid = threadIdx.x, lane = tid & 63, w = __builtin_amdgcn_readfirstlane(tid >> 6);
    const int ct = w & 3, th = w >> 2, T = 4 * slice + ct;
    unsigned* ctrl = (unsigned*)(p.ws + OFF_CTRL);
    const char* src = p.ws + OFF_XNF;
    u32x4 wf[28];
    {
        const rsrc_t rw = mk_rsrc(p.ws + OFF_WCT, (unsigned)SZ_WCT);
#pragma unroll
        for (int ks = 0; ks < 28; ++ks) wf[ks] = LD16(rw, lane * 16 + (ks & 3) * 1024, T * 28672 + (ks & ~3) * 1024, 0);
    }
#pragma unroll
    for (int i = 0; i < 7; ++i) GLDS(src + (w * 7 + i) * 1024 + lane * 16, smem + (w * 7 + i) * 1024, 0);
    asm volatile("s_waitcnt vmcnt(0)" ::: "memory");
#pragma unroll
    for (int i = 0; i < 7; ++i) GLDS(src + (size_t)57344 + (w * 7 + i) * 1024 + lane * 16, smem + 57344 + (w * 7 + i) * 1024, 0);
    const bool isK = T < 224;
    const int Th = isK ? T : T - 224, hd = Th / 14, ctile = Th % 14;
    const rsrc_t ro = mk_rsrc(p.ws + (isK ? OFF_KT : OFF_VT), (unsigned)SZ_KT);
    for (int pr = 0; pr < npair; ++pr) {
        if (pr >= 1) {
            if (pr + 1 < npair) asm volatile("s_waitcnt vmcnt(7)" ::: "memory"); else asm volatile("s_waitcnt vmcnt(0)" ::: "memory");
        }
        BAR();
        if (pr >= 2 && tid_opaque() < 2) add_agent(ctrl + CT_KV(2 * (pr - 1) + tid, slice & 7), 1u);
        const char* xs = smem + (pr & 1) * 57344 + th * 28672 + lane * 16;
        f32x16 acc;
#pragma unroll
        for (int i = 0; i < 16; ++i) acc[i] = 0.f;
        if (!isK) {
#pragma unroll
            for (int ks = 0; ks < 28; ++ks) acc = mfma32(*(const u32x4*)(xs + ks * 1024), wf[ks], acc);
        } else {
#pragma unroll
            for (int ks = 0; ks < 28; ++ks) acc = mfma32(wf[ks], *(const u32x4*)(xs + ks * 1024), acc);
        }
        const int s = 2 * pr + th;
        unsigned pk[8];
#pragma unroll
        for (int i = 0; i < 8; ++i) pk[i] = pk_bf(acc[2 * i], acc[2 * i + 1]);
        if (isK) {
#pragma unroll
            for (int k = 0; k < 2; ++k) {
                auto r0 = __builtin_amdgcn_permlane32_swap(pk[4 * k], pk[4 * k + 2], false, false);
                auto r1 = __builtin_amdgcn_permlane32_swap(pk[4 * k + 1], pk[4 * k + 3], false, false);
                u32x4 v = {r0[0], r1[0], r0[1], r1[1]};
                ST16(v, ro, lane * 16 + k * 1024, ((s * 16 + hd) * 56 + 4 * ctile) * 512, SC1);
            }
        } else {
#pragma unroll
            for (int k = 0; k < 2; ++k) {
                u32x4 v = {pk[4 * k], pk[4 * k + 1], pk[4 * k + 2], pk[4 * k + 3]};
                ST16(v, ro, lane * 16 + k * 1024, ((s * 16 + hd) * 14 + ctile) * 2048, SC1);
            }
        }
        if (pr == 0) {
            asm volatile("s_waitcnt vmcnt(0)" ::: "memory");
            BAR();
            if (tid_opaque() < 2) add_agent(ctrl + CT_KV(tid, slice & 7), 1u);
        }
        if (pr + 2 < npair) {
            BAR();
#pragma unroll
            for (int i = 0; i < 7; ++i) GLDS(src + (size_t)(pr + 2) * 57344 + (w * 7 + i) * 1024 + lane * 16, smem + (pr & 1) * 57344 + (w * 7 + i) * 1024, 0);
        }
    }
    asm volatile("s_waitcnt vmcnt(0)" ::: "memory");
    BAR();
    if (tid_opaque() < 2) add_agent(ctrl + CT_KV(2 * (npair - 1) + tid, slice & 7), 1u);
}

DI void ffn1_role(const Params& p, char* smem, int slice, int par) {
    const int tid = threadIdx.x, lane = tid & 63, w = __builtin_amdgcn_readfirstlane(tid >> 6);
    const int ct = w & 3, kh = w >> 2, T = 4 * slice + ct, hh = lane >> 5;
    unsigned* ctrl = (unsigned*)(p.ws + OFF_CTRL);
    u32x4 wf[14];
    {
#pragma unroll
        for (int i = 0; i < 14; ++i) {
            const int ch = tid + 512 * i;
            *(u32x4*)(smem + (ch >> 4) * 272 + (ch & 15) * 16) = *(const u32x4*)(p.w1 + (size_t)(ch >> 4) * FF + 128 * slice + (ch & 15) * 8);
        }
        __syncthreads();
        const int blk = (lane >> 4) & 1, a = (lane & 15) >> 2, pp = lane & 3;
#pragma unroll
        for (int ks = 0; ks < 14; ++ks) {
            const int r0 = 16 * (14 * kh + ks) + 4 * hh + a, cb = (32 * ct + 16 * blk + 4 * pp) * 2;
            const s16x4 lo = __builtin_amdgcn_ds_read_tr16_b64_v4i16((LAS s16x4*)(smem + r0 * 272 + cb));
            const s16x4 hi = __builtin_amdgcn_ds_read_tr16_b64_v4i16((LAS s16x4*)(smem + (r0 + 8) * 272 + cb));
            const u32x2 l2 = __builtin_bit_cast(u32x2, lo), h2 = __builtin_bit_cast(u32x2, hi);
            wf[ks] = (u32x4){l2.x, l2.y, h2.x, h2.y};
        }
        __syncthreads();
    }
    u32x2 bias[4];
#pragma unroll
    for (int g = 0; g < 4; ++g) bias[g] = *(const u32x2*)(p.b1 + 32 * T + 8 * g + 4 * hh);
    const rsrc_t ro = mk_rsrc(p.ws + OFF_RF, (unsigned)SZ_RF);
    char* red = smem + 28672;
    for (int s = par; s < NSEG; s += 2) {
        if (w == 0) wait_count(ctrl, CT_H(s), 4u, s >= 60 ? 0 : 4);
        BAR();
        {
            const char* src = p.ws + OFF_HNF + (size_t)s * SZ_SEGF;
#pragma unroll
            for (int i = 0; i < 4; ++i) { const int k = w + 8 * i; if (k < 28) GLDS(src + k * 1024 + lane * 16, smem + k * 1024, SC1); }
        }
        asm volatile("s_waitcnt vmcnt(0)" ::: "memory");
        BAR();
        f32x16 acc;
#pragma unroll
        for (int i = 0; i < 16; ++i) acc[i] = 0.f;
        u32x4 xf[14];
#pragma unroll
        for (int ks = 0; ks < 14; ++ks) xf[ks] = *(const u32x4*)(smem + (14 * kh + ks) * 1024 + lane * 16);
        __builtin_amdgcn_sched_barrier(0);
#pragma unroll
        for (int ks = 0; ks < 14; ++ks) acc = mfma32(wf[ks], xf[ks], acc);
        if (kh == 1) {
#pragma unroll
            for (int g = 0; g < 4; ++g) *(f32x4*)(red + ((ct * 4 + g) * 64 + lane) * 16) = (f32x4){acc[4 * g], acc[4 * g + 1], acc[4 * g + 2], acc[4 * g + 3]};
        }
        BAR();
        if (kh == 0) {
            unsigned pk[8];
#pragma unroll
            for (int g = 0; g < 4; ++g) {
                const f32x4 o2 = *(const f32x4*)(red + ((ct * 4 + g) * 64 + lane) * 16);
                const float bv[4] = {bf_lo(bias[g].x), bf_hi(bias[g].x), bf_lo(bias[g].y), bf_hi(bias[g].y)};
                float r[4];
#pragma unroll
                for (int e = 0; e < 4; ++e) { float t = rbf(rbf(acc[4 * g + e] + o2[e]) + bv[e]); t = t > 0.f ? t : 0.f; r[e] = t * t; }
                pk[2 * g] = pk_bf(r[0], r[1]); pk[2 * g + 1] = pk_bf(r[2], r[3]);
            }
#pragma unroll
            for (int k = 0; k < 2; ++k) ST16(((u32x4){pk[4 * k], pk[4 * k + 1], pk[4 * k + 2], pk[4 * k + 3]}), ro, lane * 16 + k * 1024, (s * 112 + 2 * T) * 1024, SC1);
            asm volatile("s_waitcnt vmcnt(0)" ::: "memory");
        }
        BAR();
        if (tid_opaque() == 0) add_agent(ctrl + CT_R(s), 1u);
    }
}
DI void ffn2_role(const Params& p, char* smem, int ctile, int par) {
    const int tid = threadIdx.x, lane = tid & 63, w = __builtin_amdgcn_readfirstlane(tid >> 6);
    const int tok = lane & 31, hh = lane >> 5;
    unsigned* ctrl = (unsigned*)(p.ws + OFF_CTRL);
    u32x4 wf[14];
    {
#pragma unroll
        for (int i = 0; i < 14; ++i) {
            const int ch = tid + 512 * i;
            *(u32x4*)(smem + ch * 16) = *(const u32x4*)(p.w2 + (size_t)(ch >> 2) * DM + 32 * ctile + (ch & 3) * 8);
        }
        __syncthreads();
        const int blk = (lane >> 4) & 1, a = (lane & 15) >> 2, pp = lane & 3;
#pragma unroll
        for (int ks = 0; ks < 14; ++ks) {
            const int r0 = 16 * (14 * w + ks) + 4 * hh + a, cb = (16 * blk + 4 * pp) * 2;
            const s16x4 lo = __builtin_amdgcn_ds_read_tr16_b64_v4i16((LAS s16x4*)(smem + r0 * 64 + cb));
            const s16x4 hi = __builtin_amdgcn_ds_read_tr16_b64_v4i16((LAS s16x4*)(smem + (r0 + 8) * 64 + cb));
            const u32x2 l2 = __builtin_bit_cast(u32x2, lo), h2 = __builtin_bit_cast(u32x2, hi);
            wf[ks] = (u32x4){l2.x, l2.y, h2.x, h2.y};
        }
        __syncthreads();
    }
    const u32x2 br = *(const u32x2*)(p.b2 + 32 * ctile + 8 * (w & 3) + 4 * hh);
    const rsrc_t rr = mk_rsrc(p.ws + OFF_RF, (unsigned)SZ_RF), rh = mk_rsrc(p.ws + OFF_HD, (unsigned)(64 * SZ_SEGF));
    for (int s = par; s < NSEG; s += 2) {
        if (w == 0) wait_count(ctrl, CT_R(s), 14u, s >= 60 ? 0 : 4);
        BAR();
        u32x4 xf[14];
#pragma unroll
        for (int ks = 0; ks < 14; ++ks) xf[ks] = LD16(rr, lane * 16, (s * 112 + 14 * w + ks) * 1024, SC1);
        u32x2 hr = {0u, 0u};
        if (w < 4) hr = LD8(rh, lane * 16 + (w & 1) * 8, s * (int)SZ_SEGF + (2 * ctile + (w >> 1)) * 1024, SC1);
        f32x16 acc;
#pragma unroll
        for (int i = 0; i < 16; ++i) acc[i] = 0.f;
#pragma unroll
        for (int ks = 0; ks < 14; ++ks) acc = mfma32(wf[ks], xf[ks], acc);
#pragma unroll
        for (int g = 0; g < 4; ++g) *(f32x4*)(smem + ((g * 8 + w) * 64 + lane) * 16) = (f32x4){acc[4 * g], acc[4 * g + 1], acc[4 * g + 2], acc[4 * g + 3]};
        BAR();
        if (w < 4) {
            f32x4 t[8];
#pragma unroll
            for (int k = 0; k < 8; ++k) t[k] = *(const f32x4*)(smem + ((w * 8 + k) * 64 + lane) * 16);
            const f32x4 o = ((t[0] + t[1]) + (t[2] + t[3])) + ((t[4] + t[5]) + (t[6] + t[7]));
            const float bv[4] = {bf_lo(br.x), bf_hi(br.x), bf_lo(br.y), bf_hi(br.y)};
            const float hv[4] = {bf_lo(hr.x), bf_hi(hr.x), bf_lo(hr.y), bf_hi(hr.y)};
            float y[4];
#pragma unroll
            for (int e = 0; e < 4; ++e) y[e] = hv[e] + rbf(rbf(o[e]) + bv[e]);
            *(u32x2*)(p.out + (size_t)(32 * s + tok) * DM + 32 * ctile + 8 * w + 4 * hh) = (u32x2){pk_bf(y[0], y[1]), pk_bf(y[2], y[3])};
        }
    }
}

constexpr int L_F = 0;
constexpr int L_R = 14336;
constexpr int L_MX = L_R + 1792;
constexpr int L_R2 = L_MX + 128;

constexpr unsigned POLL_CAP = 1u << 14;
DI u32x4 poll16(rsrc_t r, int vo, int so, unsigned* ctrl, unsigned& cap) {
    u32x4 v;
    unsigned sp = 0;
    bool bad;
    do {
        asm volatile("" ::: "memory");
        v = LD16(r, vo, so, SC1);
        bad = __any(max(max(v.x, v.y), max(v.z, v.w)) == 0xffffffffu);
    } while (bad && ++sp < cap);
    if (bad) { st_agent(ctrl + CT_ABORT, 1u); cap = 1u; }
    return v;
}

#define K_Y 128
#define K_X 228
#define T_START 30
#define D_Y 32
#define D_X 32
DI unsigned later(unsigned a, unsigned b) { return (int)(a - b) >= 0 ? a : b; }
DI unsigned now_ticks() { return (unsigned)__builtin_amdgcn_s_memrealtime(); }
DI void wait_until(unsigned target) {
    unsigned long long t;
    unsigned d;
    asm volatile("s_memrealtime %0\n\ts_waitcnt lgkmcnt(0)" : "=s"(t) :: "memory");
    d = target - (unsigned)t;
    if (d - 1u < 4000u) {
        do {
            asm volatile("s_memrealtime %0\n\ts_waitcnt lgkmcnt(0)" : "=s"(t) :: "memory");
        } while ((int)(target - (unsigned)t) > 0);
    }
}
constexpr int L_TX = L_R2 + 4096;

template <bool BROLE, bool W7>
DI void rec_wave(const Params& p, char* smem, const int c, const int w) {
    const int tid = threadIdx.x, lane = tid & 63;
    const int x = lane & 15, q = lane >> 4, ap = (lane >> 3) & 1, i8 = lane & 7;
    const int h = c >> 2, c4 = c & 3;
    const int u = c >= 28 ? 1 : 0, J = c - 28 * u, g = 8 * u + w;
    unsigned* ctrl = (unsigned*)(p.ws + OFF_CTRL);
    const rsrc_t rhx = mk_rsrc(p.hx, (unsigned)SZ_HX), rkt = mk_rsrc(p.ws + OFF_KT, (unsigned)SZ_KT), rvt = mk_rsrc(p.ws + OFF_VT, (unsigned)SZ_KT);
    const rsrc_t rkm = mk_rsrc(p.ws + OFF_KM, (unsigned)SZ_KM), rvm = mk_rsrc(p.ws + OFF_VM, (unsigned)SZ_KM);
    unsigned cap = POLL_CAP;

    u32x4 wA[14], wB[14];
    auto load_w = [&](u32x4 (&wt)[14], int np) {
        const rsrc_t rw = mk_rsrc(p.ws + OFF_WCT, (unsigned)SZ_WCT);
        const int vo = ((np >> 5) * 56 * 32 + q * 32 + (np & 31)) * 16;
#pragma unroll
        for (int ks = 0; ks < 14; ++ks) wt[ks] = LD16(rw, vo + (ks & 1) * 2048, (ks & ~1) * 2048, 0);
    };
    if (W7 || w != 0) {
        if (!W7) load_w(wA, h * 448 + 112 * c4 + 16 * w + x);
        if (BROLE) load_w(wB, 7168 + g * 448 + 16 * J + x);
    }
    if (!W7 && w == 0) {
        unsigned best0 = ~0u, best1 = ~0u;
        for (int rep = 0; rep < 4; ++rep) {
            const int off = c * 65536 + 16384 + (rep & 1) * 8192 + 8064;
            const unsigned long long t0 = __builtin_amdgcn_s_memtime();
            ST4((unsigned)rep, rhx, 0, off, SC1);
            asm volatile("s_waitcnt vmcnt(0)" ::: "memory");
            unsigned v = __builtin_amdgcn_raw_buffer_load_b32(rhx, 0, off, SC1);
            asm volatile("s_waitcnt vmcnt(0)" : "+v"(v) :: "memory");
            const unsigned dt = (unsigned)(__builtin_amdgcn_s_memtime() - t0);
            if (rep & 1) best1 = min(best1, dt); else best0 = min(best0, dt);
        }
        if (lane == 0) st_agent(ctrl + CT_PROBE(c), 2u | (best1 < best0 ? 1u : 0u));
        load_w(wA, h * 448 + 112 * c4 + 16 * w + x);
        if (BROLE) load_w(wB, 7168 + g * 448 + 16 * J + x);
        unsigned long long pr = 0;
        for (unsigned sp = 0; sp < SPIN_CAP; ++sp) {
            const unsigned v = ld_agent(ctrl + CT_PROBE(lane));
            pr = __ballot((v & 1u) != 0u);
            if (!__any(v == 0u)) break;
            __builtin_amdgcn_s_sleep(4);
        }
        const int cnt = __builtin_popcountll((pr >> (lane & ~7)) & 0xffull);
        pr = __ballot(cnt > 4 || (cnt == 4 && ((pr >> lane) & 1ull)));
        if (lane == 0) { *(LAS unsigned long long*)(smem + L_TX + 8) = pr; }
    }
    if (!W7) {
        const int o = tid >> 3, i = tid & 7;
        *(u32x4*)(smem + L_F + (o * 16 + i) * 16) = *(const u32x4*)(p.mem0 + i * DM + o * 8);
        *(u32x4*)(smem + L_F + (o * 16 + 8 + i) * 16) = (u32x4){0u, 0u, 0u, 0u};
    }
    BAR();
    unsigned long long probe;
    {
        const u32x2 pv = *(LAS u32x2*)(smem + L_TX + 8);
        probe = ((unsigned long long)(unsigned)__builtin_amdgcn_readfirstlane(pv.y) << 32) | (unsigned)__builtin_amdgcn_readfirstlane(pv.x);
    }
    {
        const u32x4 ones = {~0u, ~0u, ~0u, ~0u};
        const int so = c * 65536 + (int)(((probe >> c) & 1ull) << 13);
#pragma unroll
        for (int i = 0; i < 4; ++i) ST16(ones, rhx, i * 16384 + tid * 16, so, SC1);
        asm volatile("s_waitcnt vmcnt(0)" ::: "memory");
        BAR();
        if (!W7 && w == 0) {
            if (lane == 0) add_agent(ctrl + CT_START(c & 7), 1u);
            wait_shards(ctrl, CT_KV(0, 0), KV_PER_SHARD, 0);
            wait_shards(ctrl, CT_KV(1, 0), KV_PER_SHARD, 0);
            for (unsigned sp = 0; sp < SPIN_CAP; ++sp) {
                if (!__any(ld_agent(ctrl + CT_START(lane & 7)) < 8u)) break;
                __builtin_amdgcn_s_sleep(4);
            }
        }
        BAR();
    }
    auto phys = [](int lo) { return ((lo >> 13) << 14) + (lo & 8191); };
    const int vo_x0 = phys(tid * 16), vo_x1 = phys(7168 + tid * 16);
    const int lo_f = L_F + ((tid >> 3) * 16 + (tid & 7)) * 16;
    const int vo_yl = phys(16384 + (g * 4 + 2 * ap + (q >> 1)) * 256 + i8 * 32 + (q & 1) * 16);
    const int vo_ys = phys(16384 + c * 256 + lane * 4);
    const int vo_xs = phys(u * 7168 + J * 256 + lane * 4);
    const int vo_ts = phys(14336 + c * 16);
    const int vo_k = (q * 32 + 8 * c4 + i8) * 16;
    const int vo_v = ((q & 1) * 32 + 16 * (J & 1) + x) * 16 + 8 * (q >> 1);
    const int vo_km = ((14 * c4 + 2 * w + (q >> 1)) * 8 + i8) * 16 + (q & 1) * 8;
    const int vo_vm = (16 * J + x) * 16 + (q & 1) * 8;
    const int scol = 112 * c4 + 16 * w + 4 * q;
    const int lo_s = L_F + ((scol >> 3) * 16 + x) * 16 + (scol & 4) * 2;
    auto step_off = [&](int s) { return s * 65536 + (int)(((probe >> s) & 1ull) << 13); };

    u32x2 v1 = {0u, 0u}, v2 = {0u, 0u}, vn1 = {0u, 0u}, vn2 = {0u, 0u};
    auto v_load = [&](int s) {
        const int so = ((s * 16 + g) * 14 + (J >> 1)) * 2048;
        vn1 = LD8(rvt, vo_v, so, SC1); vn2 = LD8(rvt, vo_v + 1024, so, SC1);
    };
    auto k_load = [&](int s) {
#pragma unroll
        for (int ks = 0; ks < 14; ++ks) wA[ks] = LD16(rkt, vo_k, (s * 16 + h) * 28672 + ks * 2048, SC1);
    };
    if (BROLE) v_load(0);
    if (W7) k_load(0);
    u32x4 fr[14];
    int kv_ready = 2;
    const unsigned long long m_ap = __ballot(ap != 0);
    const unsigned hm = (q >> 1) ? 0xffff0000u : 0x0000ffffu, m_a = ap ? 0u : hm, m_b = ap ? hm : 0u;
    const unsigned HI = 0xffff0000u;
    const float L2E = LOG2E, NL2E = -LOG2E;
#define SB() __builtin_amdgcn_sched_barrier(0)
    const int vo_t0 = phys(14336);
    unsigned ts_val = 0xffffffffu;
    auto ts_request = [&](int so_s) { asm volatile("" ::: "memory"); ts_val = __builtin_amdgcn_raw_buffer_load_b32(rhx, vo_t0, so_s, SC1); };
    auto ts_finish = [&](int s, int so_s) {
        if (__builtin_expect(__builtin_amdgcn_readfirstlane(ts_val) == 0xffffffffu, 0)) {
            unsigned sp = 0;
            do { ts_request(so_s); } while (__builtin_amdgcn_readfirstlane(ts_val) == 0xffffffffu && ++sp < cap);
            if (sp >= cap) { st_agent(ctrl + CT_ABORT, 1u); cap = 1u; }
        }
        if (lane == 0) *(LAS unsigned*)(smem + L_TX + (s & 1) * 4) = ts_val;
    };
    auto publish_t = [&](int so_s) { if (lane == 0) ST4(min(now_ticks(), 0xfffffffeu), rhx, vo_ts, so_s, SC1); };
    unsigned x_tries = 0;
    auto gather_x = [&](int so_s) {
        auto pass = [&]() {
            asm volatile("" ::: "memory");
            const u32x4 a0 = LD16(rhx, vo_x0, so_s, SC1), a1 = LD16(rhx, vo_x1, so_s, SC1);
            *(u32x4*)(smem + lo_f) = a0;
            *(u32x4*)(smem + lo_f + 128) = a1;
            return (bool)__any(max(max(max(a0.x, a0.y), max(a0.z, a0.w)), max(max(a1.x, a1.y), max(a1.z, a1.w))) == 0xffffffffu);
        };
        x_tries = 0;
        if (__builtin_expect(pass(), 0)) {
            bool bad = true;
            while (bad && ++x_tries < cap) bad = pass();
            if (bad) { st_agent(ctrl + CT_ABORT, 1u); cap = 1u; }
        }
    };
    auto a_mfma = [&]() {
#pragma unroll
        for (int ks = 0; ks < 4; ++ks) fr[ks] = *(const u32x4*)(smem + L_F + ks * 1024 + lane * 16);
        SB();
        f32x4 acc;
        asm volatile("v_mfma_f32_16x16x32_bf16 %0, %1, %2, 0" : "=&v"(acc) : "v"(wA[0]), "v"(fr[0]));
        fr[4] = *(const u32x4*)(smem + L_F + 4 * 1024 + lane * 16);
        SB();
#pragma unroll
        for (int ks = 1; ks < 14; ++ks) {
            asm volatile("v_mfma_f32_16x16x32_bf16 %0, %1, %2, %0" : "+v"(acc) : "v"(wA[ks]), "v"(fr[ks]));
            if (ks + 4 < 14) fr[ks + 4] = *(const u32x4*)(smem + L_F + (ks + 4) * 1024 + lane * 16);
            SB();
        }
        float a0, a1, a2, a3;
        asm volatile("s_nop 7\n\t"
                     "v_add_f32_dpp %0, %4, %4 row_ror:8 row_mask:0xf bank_mask:0xf\n\tv_add_f32_dpp %1, %5, %5 row_ror:8 row_mask:0xf bank_mask:0xf\n\t"
                     "v_add_f32_dpp %2, %6, %6 row_ror:8 row_mask:0xf bank_mask:0xf\n\tv_add_f32_dpp %3, %7, %7 row_ror:8 row_mask:0xf bank_mask:0xf"
                     : "=&v"(a0), "=&v"(a1), "=&v"(a2), "=&v"(a3) : "v"(acc[0]), "v"(acc[1]), "v"(acc[2]), "v"(acc[3]));
        return (u32x2){pk_bf(a0, a1), pk_bf(a2, a3)};
    };
    auto u_part = [&]() {
        f32x4 ua = {0.f, 0.f, 0.f, 0.f};
#pragma unroll
        for (int ks = 0; ks < 14; ++ks) ua = mfma16(fr[ks], wB[ks], ua);
#pragma unroll
        for (int r = 0; r < 4; ++r) ua[r] = xsum32(ua[r]);
        return (u32x2){pk_bf(ua[0], ua[1]), pk_bf(ua[2], ua[3])};
    };

    int so_s = step_off(0);
    if (W7) {
        if (c == 0 && lane == 0) ST4(min(now_ticks() - T_START, 0xfffffffeu), rhx, vo_ts, so_s, SC1);
        ts_request(so_s);
    }
    for (int s = 0; s < NSEG - 1; ++s) {
        u32x2 tb;
        if (BROLE) { v1 = vn1; v2 = vn2; }
        BAR();
        if (BROLE) v_load(s + 1);
        if (!W7) {
            const u32x2 sop = *(const u32x2*)(smem + lo_s);
            tb = a_mfma();
            f32x4 st;
            float s0, s1, s2, s3;
            asm volatile("s_nop 1\n\tv_mfma_f32_16x16x16_bf16 %0, %1, %2, 0" : "=&v"(st) : "v"(tb), "v"(sop));
            asm volatile("s_nop 7\n\t"
                         "v_add_f32_dpp %0, %4, %4 row_ror:8 row_mask:0xf bank_mask:0xf\n\tv_add_f32_dpp %1, %5, %5 row_ror:8 row_mask:0xf bank_mask:0xf\n\t"
                         "v_add_f32_dpp %2, %6, %6 row_ror:8 row_mask:0xf bank_mask:0xf\n\tv_add_f32_dpp %3, %7, %7 row_ror:8 row_mask:0xf bank_mask:0xf"
                         : "=&v"(s0), "=&v"(s1), "=&v"(s2), "=&v"(s3) : "v"(st[0]), "v"(st[1]), "v"(st[2]), "v"(st[3]));
            if (x < 8 && q < 2) *(f32x4*)(smem + L_R + (w * 16 + 2 * x + q) * 16) = (f32x4){s0, s1, s2, s3};
        } else {
            tb = a_mfma();
            if (x < 8 && q < 2) *(u32x2*)(smem + L_MX + (2 * x + q) * 8) = tb;
            SB();
            ts_finish(s, so_s);
        }
        BAR();
        const unsigned t_b2 = now_ticks();
        if (!W7 && w == 0) {
            const int chunk = lane >> 2, part = lane & 3;
            f32x2 t[7];
#pragma unroll
            for (int i = 0; i < 7; ++i) t[i] = *(const f32x2*)(smem + L_R + (i * 16 + chunk) * 16 + (part & 1) * 8);
            unsigned mxv = *(const unsigned*)(smem + L_MX + chunk * 8 + (part & 1) * 4);
            asm volatile("" : "+v"(mxv));
            const f32x2 sum = ((t[0] + t[1]) + (t[2] + t[3])) + ((t[4] + t[5]) + t[6]);
            unsigned mmv = pk_bf(sum[0], sum[1]);
            asm volatile("" : "+v"(mmv));
            ST4(min(part < 2 ? mxv : mmv, 0xfffffffeu), rhx, vo_ys, so_s, SC1);
        }
        SB();
        const unsigned t_prev = __builtin_amdgcn_readfirstlane(*(LAS unsigned*)(smem + L_TX + (s & 1) * 4));
        const int so_n = step_off(s + 1);
        if (!W7 && s > 0 && w == 3 && tid_opaque() == 192) add_agent(ctrl + CT_M(s - 1, c & 7), 1u);
        if (!W7 && x < 8) ST8(tb, rkm, vo_km, (s * 16 + h) * 7168, SC1);
        if (W7) k_load(s + 1);
        unsigned t_x = t_prev + K_X;
        if (BROLE) {
            wait_until(later(t_prev + K_Y, t_b2 + D_Y));
            asm volatile("" ::: "memory");
            u32x4 y = LD16(rhx, vo_yl, so_s, SC1);
            SB();
            const u32x2 ub = u_part();
            SB();
            unsigned sp = 0;
            float o0, o1_, o2_, o3;
            auto pass = [&]() {
                unsigned long long bad;
                float x0, x1, x2, x3, sm, p0, p1, p2, p3, m, t;
                asm volatile(
                    "v_lshlrev_b32 %[p0], 16, %[y2]\n\tv_and_b32 %[p1], %[hi], %[y2]\n\tv_lshlrev_b32 %[p2], 16, %[y3]\n\tv_and_b32 %[p3], %[hi], %[y3]\n\t"
                    "v_lshlrev_b32 %[x0], 16, %[y0]\n\tv_and_b32 %[x1], %[hi], %[y0]\n\t"
                    "v_permlane32_swap_b32 %[p0], %[p1]\n\tv_permlane32_swap_b32 %[p2], %[p3]\n\t"
                    "v_lshlrev_b32 %[x2], 16, %[y1]\n\tv_and_b32 %[x3], %[hi], %[y1]\n\t"
                    "v_add_f32 %[p0], %[p0], %[p1]\n\tv_add_f32 %[p2], %[p2], %[p3]\n\t"
                    "v_max3_u32 %[t], %[y0], %[y1], %[y2]\n\t"
                    "v_cndmask_b32 %[p1], %[p2], %[p0], %[ap]\n\tv_cndmask_b32 %[p3], %[p0], %[p2], %[ap]\n\t"
                    "v_max3_f32 %[m], %[x0], %[x1], %[x2]\n\t"
                    "v_max_u32 %[t], %[t], %[y3]\n\t"
                    "v_add_f32_dpp %[sm], %[p1], %[p3] row_ror:8 row_mask:0xf bank_mask:0xf\n\t"
                    "v_cmp_eq_u32 %[bad], -1, %[t]\n\t"
                    "v_max3_f32 %[m], %[m], %[x3], %[sm]\n\t"
                    "s_nop 1\n\t"
                    "v_max_f32_dpp %[m], %[m], %[m] row_ror:8 row_mask:0xf bank_mask:0xf\n\t"
                    "v_mov_b32 %[t], %[m]\n\ts_nop 1\n\tv_permlane16_swap_b32 %[m], %[t]\n\tv_max_f32 %[m], %[m], %[t]\n\t"
                    "v_mov_b32 %[t], %[m]\n\ts_nop 1\n\tv_permlane32_swap_b32 %[m], %[t]\n\tv_max_f32 %[m], %[m], %[t]\n\t"
                    "v_mul_f32 %[m], %[nl2e], %[m]\n\t"
                    "v_fma_f32 %[sm], %[sm], %[l2e], %[m]\n\tv_fma_f32 %[x0], %[x0], %[l2e], %[m]\n\tv_fma_f32 %[x1], %[x1], %[l2e], %[m]\n\t"
                    "v_fma_f32 %[x2], %[x2], %[l2e], %[m]\n\tv_fma_f32 %[x3], %[x3], %[l2e], %[m]\n\t"
                    "v_exp_f32 %[sm], %[sm]\n\tv_exp_f32 %[x0], %[x0]\n\tv_exp_f32 %[x1], %[x1]\n\tv_exp_f32 %[x2], %[x2]\n\tv_exp_f32 %[x3], %[x3]\n\ts_nop 0"
                    : [x0] "=&v"(x0), [x1] "=&v"(x1), [x2] "=&v"(x2), [x3] "=&v"(x3), [sm] "=&v"(sm), [p0] "=&v"(p0), [p1] "=&v"(p1), [p2] "=&v"(p2), [p3] "=&v"(p3),
                      [m] "=&v"(m), [t] "=&v"(t), [bad] "=&s"(bad)
                    : [y0] "v"(y.x), [y1] "v"(y.y), [y2] "v"(y.z), [y3] "v"(y.w), [hi] "s"(HI), [ap] "s"(m_ap), [l2e] "s"(L2E), [nl2e] "s"(NL2E));
                const unsigned tm = pk_bf(sm, sm);
                const u32x2 pb = {pk_bf(x0, x1), pk_bf(x2, x3)};
                const u32x2 pm = {tm & m_a, tm & m_b};
                f32x4 om, oa, ob;
                asm volatile("s_nop 1\n\tv_mfma_f32_16x16x16_bf16 %0, %3, %4, 0\n\tv_mfma_f32_16x16x16_bf16 %1, %5, %7, %0\n\tv_mfma_f32_16x16x16_bf16 %2, %6, %7, %0"
                             : "=&v"(om), "=&v"(oa), "=&v"(ob) : "v"(ub), "v"(pm), "v"(v1), "v"(v2), "v"(pb));
                float l, l2;
                asm volatile(
                    "v_add_f32 %[l], %[x0], %[x1]\n\tv_add_f32 %[l2], %[x2], %[x3]\n\tv_add_f32 %[l], %[l], %[sm]\n\tv_add_f32 %[l], %[l], %[l2]\n\ts_nop 1\n\t"
                    "v_add_f32_dpp %[l], %[l], %[l] row_ror:8 row_mask:0xf bank_mask:0xf\n\t"
                    "v_mov_b32 %[l2], %[l]\n\ts_nop 1\n\tv_permlane16_swap_b32 %[l], %[l2]\n\tv_add_f32 %[l], %[l], %[l2]\n\t"
                    "v_mov_b32 %[l2], %[l]\n\ts_nop 1\n\tv_permlane32_swap_b32 %[l], %[l2]\n\tv_add_f32 %[l], %[l], %[l2]\n\t"
                    "v_rcp_f32 %[l], %[l]\n\t"
                    "v_add_f32_dpp %[r0], %[b0], %[a0] row_ror:8 row_mask:0xf bank_mask:0xf\n\tv_add_f32_dpp %[r1], %[b1], %[a1] row_ror:8 row_mask:0xf bank_mask:0xf\n\t"
                    "v_add_f32_dpp %[r2], %[b2], %[a2] row_ror:8 row_mask:0xf bank_mask:0xf\n\tv_add_f32_dpp %[r3], %[b3], %[a3] row_ror:8 row_mask:0xf bank_mask:0xf\n\t"
                    "v_mul_f32 %[r0], %[r0], %[l]\n\tv_mul_f32 %[r1], %[r1], %[l]\n\tv_mul_f32 %[r2], %[r2], %[l]\n\tv_mul_f32 %[r3], %[r3], %[l]"
                    : [l] "=&v"(l), [l2] "=&v"(l2), [r0] "=&v"(o0), [r1] "=&v"(o1_), [r2] "=&v"(o2_), [r3] "=&v"(o3)
                    : [x0] "v"(x0), [x1] "v"(x1), [x2] "v"(x2), [x3] "v"(x3), [sm] "v"(sm),
                      [a0] "v"(oa[0]), [a1] "v"(oa[1]), [a2] "v"(oa[2]), [a3] "v"(oa[3]), [b0] "v"(ob[0]), [b1] "v"(ob[1]), [b2] "v"(ob[2]), [b3] "v"(ob[3]));
                return bad != 0;
            };
            if (__builtin_expect(pass(), 0)) {
                bool bad = true;
                while (bad && ++sp < cap) { asm volatile("" ::: "memory"); y = LD16(rhx, vo_yl, so_s, SC1); bad = pass(); }
                if (bad) { st_agent(ctrl + CT_ABORT, 1u); cap = 1u; }
            }
            const f32x4 o = {o0, o1_, o2_, o3};
            if (x < 8) *(f32x4*)(smem + L_R2 + ((w * 8 + x) * 4 + q) * 16) = o;
            BAR();
            t_x = later(t_x, now_ticks() + D_X);
            if (W7) {
                const int hf = lane >> 5, i = (lane >> 2) & 7, d = lane & 3;
                f32x2 t[8];
#pragma unroll
                for (int k = 0; k < 8; ++k) t[k] = *(const f32x2*)(smem + L_R2 + ((k * 8 + i) * 4 + 2 * hf + (d >> 1)) * 16 + (d & 1) * 8);
                const f32x2 sum = ((t[0] + t[1]) + (t[2] + t[3])) + ((t[4] + t[5]) + (t[6] + t[7]));
                ST4(min(pk_bf(sum[0], sum[1]), 0xfffffffeu), rhx, vo_xs, so_n, SC1);
                if (c == 0) publish_t(so_n);
            }
            SB();
            if (q < 2) ST8(ub, rvm, vo_vm, (s * 16 + g) * 7168, SC1);
        }
        if (!BROLE) {
            wait_until(later(t_prev + K_Y, t_b2 + D_Y));
            (void)poll16(rhx, vo_yl, so_s, ctrl, cap);
            t_x = later(t_x, now_ticks() + 30 + D_X);
        }
        if (W7) {
            if (s + 2 < NSEG && s + 2 >= kv_ready) {
                for (unsigned sp = 0; sp < SPIN_CAP; ++sp) {
                    const int sg = s + 2 + (lane >> 3);
                    const unsigned long long rdy = __ballot(sg >= NSEG || ld_agent(ctrl + CT_KV(sg & 63, lane & 7)) >= KV_PER_SHARD);
                    kv_ready = s + 2 + ((rdy == ~0ull ? 64 : __builtin_ctzll(~rdy)) >> 3);
                    if (s + 2 < kv_ready) break;
                    __builtin_amdgcn_s_sleep(4);
                    if (sp + 1 == SPIN_CAP) st_agent(ctrl + CT_ABORT, 1u);
                }
            }
            wait_until(t_x);
            ts_request(so_n);
        } else {
            wait_until(t_x);
            gather_x(so_n);
        }
        so_s = so_n;
    }
    {
        const int s = NSEG - 1;
        if (W7) asm volatile("s_waitcnt vmcnt(0)" ::: "memory");
        BAR();
        if (!W7 && tid_opaque() == 0) add_agent(ctrl + CT_M(s - 1, c & 7), 1u);
        if (!W7) {
            const u32x2 tb = a_mfma();
            if (x < 8) ST8(tb, rkm, vo_km, (s * 16 + h) * 7168, SC1);
        } else {
#pragma unroll
            for (int ks = 0; ks < 14; ++ks) fr[ks] = *(const u32x4*)(smem + L_F + ks * 1024 + lane * 16);
        }
        if (BROLE) { const u32x2 ub = u_part(); if (q < 2) ST8(ub, rvm, vo_vm, (s * 16 + g) * 7168, SC1); }
        asm volatile("s_waitcnt vmcnt(0)" ::: "memory");
        BAR();
        if (!W7 && tid_opaque() == 0) add_agent(ctrl + CT_M(s, c & 7), 1u);
    }
}

DI void rec_role(const Params& p, char* smem, int c) {
    const int w = __builtin_amdgcn_readfirstlane(threadIdx.x >> 6);
    if (c < 56) { if (w < 7) rec_wave<true, false>(p, smem, c, w); else rec_wave<true, true>(p, smem, c, w); }
    else        { if (w < 7) rec_wave<false, false>(p, smem, c, w); else rec_wave<false, true>(p, smem, c, w); }
}

constexpr int LA_X = 0;
constexpr int LA_SM = 28672;
constexpr int LA_PX = LA_SM + 16384;
constexpr int LA_PM = LA_PX + 8192;
constexpr int LA_RED = LA_PM + 2048;
constexpr int LA_BC = LA_RED + 1024;

DI void attn_item(const Params& p, char* smem, int s, int hg) {
    const int tid = threadIdx.x, lane = tid & 63, w = __builtin_amdgcn_readfirstlane(tid >> 6);
    const int tok = lane & 31, hh = lane >> 5;
    unsigned* ctrl = (unsigned*)(p.ws + OFF_CTRL);
    const rsrc_t rkt = mk_rsrc(p.ws + OFF_KT, (unsigned)SZ_KT), rvt = mk_rsrc(p.ws + OFF_VT, (unsigned)SZ_KT);
    const rsrc_t rkm = mk_rsrc(p.ws + OFF_KM, (unsigned)SZ_KM), rvm = mk_rsrc(p.ws + OFF_VM, (unsigned)SZ_KM);
    const rsrc_t rop = mk_rsrc(p.ws + OFF_OP, (unsigned)SZ_OP);
    const rsrc_t rhd = mk_rsrc(p.ws + OFF_HD, (unsigned)(64 * SZ_SEGF)), rhn = mk_rsrc(p.ws + OFF_HNF, (unsigned)(64 * SZ_SEGF));
    {
        const char* src = p.ws + OFF_XNF + (size_t)s * SZ_SEGF;
#pragma unroll
        for (int i = 0; i < 4; ++i) { const int k = w + 8 * i; if (k < 28) GLDS(src + k * 1024 + lane * 16, smem + LA_X + k * 1024, 0); }
    }
    if (w == 0) wait_shards(ctrl, CT_KV(s, 0), KV_PER_SHARD, 2);
    asm volatile("s_waitcnt vmcnt(0)" ::: "memory");
    BAR();
    const int nt = w < 6 ? 2 : 1;
    f32x16 sx;
#pragma unroll
    for (int i = 0; i < 16; ++i) sx[i] = 0.f;
    if (w < 4) {
        u32x4 kf[28];
#pragma unroll
        for (int ks = 0; ks < 28; ++ks) kf[ks] = LD16(rkt, lane * 16, (s * 16 + 4 * hg + w) * 28672 + ks * 1024, SC1);
#pragma unroll
        for (int ks = 0; ks < 28; ++ks) sx = mfma32(kf[ks], *(const u32x4*)(smem + LA_X + ks * 1024 + lane * 16), sx);
    }
    u32x4 vf[2][8];
#pragma unroll
    for (int t = 0; t < 2; ++t)
        if (t < nt) {
#pragma unroll
            for (int k = 0; k < 8; ++k) vf[t][k] = LD16(rvt, lane * 16, (((s * 16 + 4 * hg + (k >> 1)) * 14 + w + 8 * t) * 2 + (k & 1)) * 1024, SC1);
        }
    if (w == 4) wait_shards(ctrl, CT_M(s, 0), M_PER_SHARD, s < 60 ? 4 : 1);
    BAR();
    u32x2 vm[2][4];
#pragma unroll
    for (int t = 0; t < 2; ++t)
        if (t < nt) {
#pragma unroll
            for (int k = 0; k < 4; ++k) vm[t][k] = LD8(rvm, (32 * (w + 8 * t) + tok) * 16 + hh * 8, (s * 16 + 4 * hg + k) * 7168, SC1);
        }
    if (w >= 4) {
        const int kq = w - 4;
        u32x4 mf[7];
#pragma unroll
        for (int i = 0; i < 7; ++i) mf[i] = LD16(rkm, (hh * 8 + (tok & 7)) * 16 + (tok >> 3) * 7168, (s * 16 + 4 * hg) * 7168 + (7 * kq + i) * 256, SC1);
        f32x16 sm;
#pragma unroll
        for (int i = 0; i < 16; ++i) sm[i] = 0.f;
#pragma unroll
        for (int i = 0; i < 7; ++i) sm = mfma32(mf[i], *(const u32x4*)(smem + LA_X + (7 * kq + i) * 1024 + lane * 16), sm);
#pragma unroll
        for (int gq = 0; gq < 4; ++gq) *(f32x4*)(smem + LA_SM + ((kq * 4 + gq) * 64 + lane) * 16) = (f32x4){sm[4 * gq], sm[4 * gq + 1], sm[4 * gq + 2], sm[4 * gq + 3]};
    }
    BAR();
    if (w < 4) {
        f32x4 m4 = *(const f32x4*)(smem + LA_SM + ((0 * 4 + w) * 64 + lane) * 16);
#pragma unroll
        for (int kq = 1; kq < 4; ++kq) m4 += *(const f32x4*)(smem + LA_SM + ((kq * 4 + w) * 64 + lane) * 16);
        float mx = fmaxf(fmaxf(m4[0], m4[1]), fmaxf(m4[2], m4[3]));
#pragma unroll
        for (int i = 0; i < 16; ++i) {
            const int key = (i & 3) + 8 * (i >> 2) + 4 * hh;
            sx[i] = key <= tok ? sx[i] : -1e30f;
            mx = fmaxf(mx, sx[i]);
        }
        mx = xmax32(mx);
        const float mc = mx * LOG2E;
        float l = 0.f;
#pragma unroll
        for (int i = 0; i < 16; ++i) { sx[i] = __builtin_amdgcn_exp2f(sx[i] * LOG2E - mc); l += sx[i]; }
#pragma unroll
        for (int i = 0; i < 4; ++i) { m4[i] = __builtin_amdgcn_exp2f(m4[i] * LOG2E - mc); l += m4[i]; }
        l = xsum32(l);
        const float inv = frcp(l);
#pragma unroll
        for (int k = 0; k < 2; ++k) {
            u32x4 v = {pk_bf(sx[8 * k] * inv, sx[8 * k + 1] * inv), pk_bf(sx[8 * k + 2] * inv, sx[8 * k + 3] * inv),
                       pk_bf(sx[8 * k + 4] * inv, sx[8 * k + 5] * inv), pk_bf(sx[8 * k + 6] * inv, sx[8 * k + 7] * inv)};
            *(u32x4*)(smem + LA_PX + ((w * 2 + k) * 64 + lane) * 16) = v;
        }
        *(u32x2*)(smem + LA_PM + (w * 64 + lane) * 8) = (u32x2){pk_bf(m4[0] * inv, m4[1] * inv), pk_bf(m4[2] * inv, m4[3] * inv)};
    }
    BAR();
#pragma unroll
    for (int t = 0; t < 2; ++t)
        if (t < nt) {
            f32x16 o;
#pragma unroll
            for (int i = 0; i < 16; ++i) o[i] = 0.f;
#pragma unroll
            for (int k = 0; k < 8; ++k) o = mfma32(vf[t][k], *(const u32x4*)(smem + LA_PX + (k * 64 + lane) * 16), o);
#pragma unroll
            for (int pp = 0; pp < 2; ++pp) {
                const u32x2 pa = *(const u32x2*)(smem + LA_PM + ((2 * pp) * 64 + lane) * 8), pb = *(const u32x2*)(smem + LA_PM + ((2 * pp + 1) * 64 + lane) * 8);
                o = mfma32((u32x4){vm[t][2 * pp].x, vm[t][2 * pp].y, vm[t][2 * pp + 1].x, vm[t][2 * pp + 1].y}, (u32x4){pa.x, pa.y, pb.x, pb.y}, o);
            }
#pragma unroll
            for (int gq = 0; gq < 4; ++gq) {
                f32x4 v = {o[4 * gq], o[4 * gq + 1], o[4 * gq + 2], o[4 * gq + 3]};
                ST16(__builtin_bit_cast(u32x4, v), rop, lane * 16 + gq * 1024, ((s * 4 + hg) * 14 + w + 8 * t) * 4096, SC1);
            }
        }
    asm volatile("s_waitcnt vmcnt(0)" ::: "memory");
    BAR();
    if (w == 0) {
        if (lane == 0) add_agent(ctrl + CT_A(s), 1u);
        wait_count(ctrl, CT_A(s), 4u, 0);
    }
    BAR();
    {
        const int t8 = tid & 7, pr = tid >> 3, tk = 8 * hg + t8;
        const int cti = pr >> 2, k = (pr >> 1) & 1, h2 = pr & 1;
        const bool act = tid < 448;
        float hv[8];
        float ssum = 0.f;
        if (act) {
            const int vo = ((cti * 4 + 2 * k) * 64 + h2 * 32 + tk) * 16;
            f32x4 pa[4], pb[4];
#pragma unroll
            for (int g4 = 0; g4 < 4; ++g4) {
                pa[g4] = __builtin_bit_cast(f32x4, LD16(rop, vo, (s * 4 + g4) * 57344, SC1));
                pb[g4] = __builtin_bit_cast(f32x4, LD16(rop, vo + 1024, (s * 4 + g4) * 57344, SC1));
            }
            const uint16_t* xp = p.x + (size_t)(32 * s + tk) * DM + 32 * cti + 16 * k + 4 * h2;
            const u32x2 xa = *(const u32x2*)xp, xb = *(const u32x2*)(xp + 8);
            const f32x4 a = ((pa[0] + pa[1]) + pa[2]) + pa[3], bq = ((pb[0] + pb[1]) + pb[2]) + pb[3];
            const float xv[8] = {bf_lo(xa.x), bf_hi(xa.x), bf_lo(xa.y), bf_hi(xa.y), bf_lo(xb.x), bf_hi(xb.x), bf_lo(xb.y), bf_hi(xb.y)};
#pragma unroll
            for (int e = 0; e < 4; ++e) { hv[e] = rbf(xv[e] + rbf(a[e])); hv[4 + e] = rbf(xv[4 + e] + rbf(bq[e])); }
#pragma unroll
            for (int e = 0; e < 8; ++e) ssum += hv[e];
        } else {
#pragma unroll
            for (int e = 0; e < 8; ++e) hv[e] = 0.f;
        }
        float* red = (float*)(smem + LA_RED);
        ssum += dpp_ror8(ssum); ssum = xsum32(xsum16(ssum));
        if (lane < 8) red[w * 8 + lane] = ssum;
        BAR();
        float mean = 0.f;
#pragma unroll
        for (int q8 = 0; q8 < 7; ++q8) mean += red[q8 * 8 + t8];
        mean *= (1.0f / DM);
        float s2 = 0.f;
        if (act) {
#pragma unroll
            for (int e = 0; e < 8; ++e) { const float d = hv[e] - mean; s2 += d * d; }
        }
        s2 += dpp_ror8(s2); s2 = xsum32(xsum16(s2));
        if (lane < 8) red[64 + w * 8 + lane] = s2;
        BAR();
        float var = 0.f;
#pragma unroll
        for (int q8 = 0; q8 < 7; ++q8) var += red[64 + q8 * 8 + t8];
        var *= (1.0f / DM);
        const float mb = rbf(mean), den = rbf(sqrtf(rbf(rbf(var) + 1e-5f)));
        if (act) {
            float y[8];
            const float rden = frcp(den);
#pragma unroll
            for (int e = 0; e < 8; ++e) y[e] = fdiv(rbf(hv[e] - mb), rden);
            const int vo = ((2 * cti + k) * 64 + h2 * 32 + tk) * 16;
            ST16(((u32x4){pk_bf(hv[0], hv[1]), pk_bf(hv[2], hv[3]), pk_bf(hv[4], hv[5]), pk_bf(hv[6], hv[7])}), rhd, vo, s * (int)SZ_SEGF, SC1);
            ST16(((u32x4){pk_bf(y[0], y[1]), pk_bf(y[2], y[3]), pk_bf(y[4], y[5]), pk_bf(y[6], y[7])}), rhn, vo, s * (int)SZ_SEGF, SC1);
        }
        asm volatile("s_waitcnt vmcnt(0)" ::: "memory");
        BAR();
        if (tid_opaque() == 0) add_agent(ctrl + CT_H(s), 1u);
    }
    BAR();
}

constexpr int K2_BC = 122880;
constexpr int K2_LDS = K2_BC + 256;

__global__ void __launch_bounds__(512, 2) k2_main(Params p) {
    extern __shared__ __attribute__((aligned(16))) char smem[];
    const int tid = threadIdx.x;
    unsigned* ctrl = (unsigned*)(p.ws + OFF_CTRL);
    volatile LAS unsigned* bc = (volatile LAS unsigned*)(smem + K2_BC);
    if (tid == 0) {
        const unsigned xc = xcc_id();
        const unsigned r = add_agent(ctrl + CT_TICK(xc), 1u);
        unsigned role;
        if ((xc == 0u || xc == 2u) && r < 32u) role = (xc >> 1) * 32u + r;
        else role = N_REC + add_agent(ctrl + CT_WTICK, 1u);
        asm volatile("s_waitcnt vmcnt(0)" ::: "memory");
        add_agent(ctrl + CT_TOTAL, 1u);
        if (role >= N_WG) {
            unsigned sp = 0;
            while (ld_agent(ctrl + CT_TOTAL) < gridDim.x && ++sp < SPIN_CAP) __builtin_amdgcn_s_sleep(8);
            const unsigned c0 = min(ld_agent(ctrl + CT_TICK(0)), 32u), c2 = min(ld_agent(ctrl + CT_TICK(2)), 32u);
            const unsigned mi = role - N_WG;
            role = mi < 32u - c0 ? c0 + mi : 32u + c2 + (mi - (32u - c0));
        }
        bc[0] = role;
    }
    __syncthreads();
    const int role = __builtin_amdgcn_readfirstlane(bc[0]);
    __syncthreads();
    if (role < N_REC) { rec_role(p, smem, role); return; }
    if (role < N_REC + N_GEMM) { proj_role(p, smem, role - N_REC); __syncthreads(); }
    else if (role < N_REC + N_GEMM + N_F1) { const int i = role - N_REC - N_GEMM; ffn1_role(p, smem, i >> 1, i & 1); return; }
    else if (role < N_REC + N_GEMM + N_F1 + N_F2) { const int i = role - N_REC - N_GEMM - N_F1; ffn2_role(p, smem, i >> 1, i & 1); return; }
    for (;;) {
        if (tid_opaque() == 0) bc[0] = add_agent(ctrl + CT_ATICK, 1u);
        __syncthreads();
        const int item = __builtin_amdgcn_readfirstlane(bc[0]);
        __syncthreads();
        if (item >= 4 * NSEG) break;
        attn_item(p, smem, item >> 2, item & 3);
    }
}

extern "C" void kernel_launch(void* const* d_in, const int* in_sizes, int n_in, void* d_out, int out_size, void* d_ws, size_t ws_size, hipStream_t stream) {
    Params p;
    p.x = (const uint16_t*)d_in[0]; p.mem0 = (const uint16_t*)d_in[1];
    p.wq = (const uint16_t*)d_in[2]; p.wk = (const uint16_t*)d_in[3]; p.wv = (const uint16_t*)d_in[4]; p.wo = (const uint16_t*)d_in[5];
    p.w1 = (const uint16_t*)d_in[8]; p.b1 = (const uint16_t*)d_in[9]; p.w2 = (const uint16_t*)d_in[10]; p.b2 = (const uint16_t*)d_in[11];
    p.out = (uint16_t*)d_out; p.ws = (char*)d_ws;
    uintptr_t hx = ((uintptr_t)d_ws + OFF_END + ((size_t)2 << 20) - 1) & ~(uintptr_t)(((size_t)2 << 20) - 1);
    p.hx = (char*)hx;
    (void)hipFuncSetAttribute((const void*)k2_main, hipFuncAttributeMaxDynamicSharedMemorySize, K2_LDS);
    (void)hipFuncSetAttribute((const void*)k1_prep, hipFuncAttributeMaxDynamicSharedMemorySize, K1_LDS);
    hipLaunchKernelGGL(k1_prep, dim3(K1_GRID), dim3(256), K1_LDS, stream, p);
    hipLaunchKernelGGL(k2_main, dim3(N_WG), dim3(512), K2_LDS, stream, p);
}
```
